```python
import torch

def _softmax(x, axis=-1):
    return torch.softmax(x, dim=axis)
def _gelu(x):
    return torch.nn.functional.gelu(x, approximate="tanh")
def _silu(x):
    return torch.nn.functional.silu(x)
def _sigmoid(x):
    return torch.sigmoid(x)
def _layernorm(x, axis=-1, eps=1e-5):
    m = x.mean(dim=axis, keepdim=True)
    v = x.var(dim=axis, keepdim=True, unbiased=False)
    return (x - m) / torch.sqrt(v + eps)
def _scatter_add(tgt, idx, src, axis=0):
    return tgt.clone().index_add_(axis, idx, src)
def _segment_sum(src, seg, n):
    out = torch.zeros((n,) + src.shape[1:], dtype=src.dtype)
    return out.index_add_(0, seg, src)
def _pad(x, pw):
    flat = tuple(v for pair in reversed(pw) for v in pair)
    return torch.nn.functional.pad(x, flat)

kernel_name = 'interleave_layers__ssm_conv_h3__glu_gated_glu__prompt'

def setup_inputs():

    _w = (lambda a: (lambda t: t.to(torch.bfloat16) if t.is_floating_point() else t)(torch.as_tensor(a)))
    def _A_setup():
        B, S, D = 1, 2048, 640
        ks = 4
        return {
            "q": _w((lambda *s: torch.randn(*s, dtype=torch.bfloat16) * 0.05)(B, S, D)),
            "k": _w((lambda *s: torch.randn(*s, dtype=torch.bfloat16) * 0.4)(B, S, D)),
            "v": _w((lambda *s: torch.randn(*s, dtype=torch.bfloat16) * 0.4)(B, S, D)),
            "shift_k": _w((lambda *s: torch.randn(*s, dtype=torch.bfloat16) * 0.4)(D, ks) * 0.5),
            "ssm_k": _w((lambda *s: torch.randn(*s, dtype=torch.bfloat16) * 0.4)(D, S) * (1.0 / S ** 0.5)),
            "ssm_D": _w((lambda *s: torch.randn(*s, dtype=torch.bfloat16) * 0.4)(D)),
        }
    _A_in = _A_setup()

    def _B_setup():
        B, S, D, F = 1, 2048, 640, 1728
        return {
            "x": _w((lambda *s: torch.randn(*s, dtype=torch.bfloat16) * 0.05)(B, S, D)),
            "w_gate": _w((lambda *s: torch.randn(*s, dtype=torch.bfloat16) * 0.05)(D, F)),
            "w_up": _w((lambda *s: torch.randn(*s, dtype=torch.bfloat16) * 0.05)(D, F)),
            "w_down": _w((lambda *s: torch.randn(*s, dtype=torch.bfloat16) * 0.05)(F, D)),
        }
    _B_in = _B_setup()

    return {
        "A_q": _A_in["q"],
        "A_k": _A_in["k"],
        "A_v": _A_in["v"],
        "A_shift_k": _A_in["shift_k"],
        "A_ssm_k": _A_in["ssm_k"],
        "A_ssm_D": _A_in["ssm_D"],
        "B_x": _B_in["x"],
        "B_w_gate": _B_in["w_gate"],
        "B_w_up": _B_in["w_up"],
        "B_w_down": _B_in["w_down"],
    }

def reference(A_q, A_k, A_v, A_shift_k, A_ssm_k, A_ssm_D, B_x, B_w_gate, B_w_up, B_w_down):
    def _A_ref(q, k, v, shift_k, ssm_k, ssm_D):
        B, S, D = q.shape
        ks = shift_k.shape[-1]
        k_pad = _pad(k, ((0, 0), (ks - 1, 0), (0, 0)))
        k_shift = torch.zeros_like(k)
        for t in range(ks):
            k_shift = k_shift + k_pad[:, t:t + S, :] * shift_k[:, ks - 1 - t][None, None, :]
        kv = k_shift * v
        L = 2 * S
        kv_pad = _pad(kv, ((0, 0), (0, S), (0, 0)))
        kf_pad = _pad(ssm_k, ((0, 0), (0, S)))
        kv_f = (lambda x, axes=(-2,-1): torch.fft.rfft2(x.float(), dim=axes))(kv_pad[:, None, :, :], axes=(1, 2))
        kf_f = (lambda x, axes=(-2,-1): torch.fft.rfft2(x.float(), dim=axes))(kf_pad[:, None, :], axes=(1, 2))
        kf_f = (lambda x, *d: x.permute(*d))(kf_f, 1, 2, 0)
        y_f = kv_f * kf_f[None, :, :, :]
        y = (lambda x, s, axes=(-2,-1): torch.fft.irfft2(x, s=s, dim=axes).to(torch.bfloat16))(y_f, (1, L), axes=(1, 2))[:, 0, :S, :]
        y = y + kv * ssm_D[None, None, :]
        return q * y

    def _B_ref(x, w_gate, w_up, w_down):
        g = torch.einsum("bsd,df->bsf", x, w_gate)
        u = torch.einsum("bsd,df->bsf", x, w_up)
        h = _sigmoid(g) * u
        return torch.einsum("bsf,fd->bsd", h, w_down)

    x = A_q
    x = x + _A_ref(_layernorm(x, axis=-1), A_k, A_v, A_shift_k, A_ssm_k, A_ssm_D)
    x = x + _B_ref(_layernorm(x, axis=-1), B_w_gate, B_w_up, B_w_down)
    x = x + _A_ref(_layernorm(x, axis=-1), A_k, A_v, A_shift_k, A_ssm_k, A_ssm_D)
    x = x + _B_ref(_layernorm(x, axis=-1), B_w_gate, B_w_up, B_w_down)
    return x
```

```cpp
#include <hip/hip_runtime.h>
#include <hip/hip_bf16.h>

typedef unsigned short u16;
typedef __attribute__((ext_vector_type(4))) short s16x4;
typedef __attribute__((ext_vector_type(8))) short s16x8;
typedef __attribute__((ext_vector_type(16))) float f32x16;
typedef __attribute__((ext_vector_type(4))) float f32x4;
typedef __attribute__((ext_vector_type(4))) unsigned u32x4;
typedef __attribute__((ext_vector_type(2))) unsigned u32x2;
typedef __attribute__((address_space(3))) s16x4* lds_s16x4_ptr;

constexpr int S = 2048, D = 640, F = 1728;

__device__ __forceinline__ float bf2f(u16 b) { return __uint_as_float((unsigned)b << 16); }
__device__ __forceinline__ float bflo(unsigned p) { return __uint_as_float(p << 16); }
__device__ __forceinline__ float bfhi(unsigned p) { return __uint_as_float(p & 0xffff0000u); }
__device__ __forceinline__ u16 f2bf(float x) { __bf16 b = (__bf16)x; return __builtin_bit_cast(u16, b); }
__device__ __forceinline__ float rbf(float x) { return bf2f(f2bf(x)); }
__device__ __forceinline__ unsigned pack2(float lo, float hi) { return (unsigned)f2bf(lo) | ((unsigned)f2bf(hi) << 16); }

#define WAIT_V(n) asm volatile("s_waitcnt vmcnt(%0)" ::"n"(n) : "memory")
#define WAIT_L(n) asm volatile("s_waitcnt lgkmcnt(%0)" ::"n"(n) : "memory")

__global__ __launch_bounds__(256) void k_prep(const u16* __restrict__ k, const u16* __restrict__ v,
                                              const u16* __restrict__ shift_k, u16* __restrict__ kvT) {
  __shared__ __attribute__((aligned(16))) u16 tile[64][72];
  const int t0 = blockIdx.x * 64, d0 = blockIdx.y * 64;
  const int tid = threadIdx.x;
  const int cd = (tid & 7) * 8, rt = tid >> 3;
  float w[8][4];
  {
    const u32x4* sp = (const u32x4*)(shift_k + (size_t)(d0 + cd) * 4);
#pragma unroll
    for (int i = 0; i < 4; ++i) {
      u32x4 q = sp[i];
      w[2 * i][0] = bflo(q[0]); w[2 * i][1] = bfhi(q[0]); w[2 * i][2] = bflo(q[1]); w[2 * i][3] = bfhi(q[1]);
      w[2 * i + 1][0] = bflo(q[2]); w[2 * i + 1][1] = bfhi(q[2]); w[2 * i + 1][2] = bflo(q[3]); w[2 * i + 1][3] = bfhi(q[3]);
    }
  }
#pragma unroll
  for (int pass = 0; pass < 2; ++pass) {
    const int tl = rt + 32 * pass, t = t0 + tl;
    u32x4 kk[4];
#pragma unroll
    for (int j = 0; j < 4; ++j) {
      u32x4 z = {0u, 0u, 0u, 0u};
      kk[j] = (t - j >= 0) ? *(const u32x4*)(k + (size_t)(t - j) * D + d0 + cd) : z;
    }
    u32x4 vv = *(const u32x4*)(v + (size_t)t * D + d0 + cd);
#pragma unroll
    for (int e = 0; e < 8; ++e) {
      float ks = 0.f;
#pragma unroll
      for (int j = 3; j >= 0; --j) {
        unsigned p = kk[j][e >> 1];
        float kf = (e & 1) ? bfhi(p) : bflo(p);
        ks = rbf(ks + rbf(kf * w[e][j]));
      }
      unsigned p = vv[e >> 1];
      float vf = (e & 1) ? bfhi(p) : bflo(p);
      tile[cd + e][tl] = f2bf(ks * vf);
    }
  }
  __syncthreads();
  {
    const int dl = tid >> 2, c = (tid & 3) * 16;
    u32x4 a = *(const u32x4*)&tile[dl][c];
    u32x4 b = *(const u32x4*)&tile[dl][c + 8];
    u16* dst = kvT + (size_t)(d0 + dl) * S + t0 + c;
    *(u32x4*)dst = a;
    *(u32x4*)(dst + 8) = b;
  }
}

constexpr int CV_T_BYTES = 2112 * 8;
constexpr int CV_U_OFF = 32768;
constexpr int CV_U_ROW = 144;
constexpr int CV_LDS = CV_U_OFF + 64 * CV_U_ROW;

__global__ __launch_bounds__(256) void k_conv(const u16* __restrict__ kvT, const u16* __restrict__ ssm_k,
                                              const u16* __restrict__ ssm_D, u16* __restrict__ YT) {
  __shared__ __attribute__((aligned(16))) char lds[CV_LDS];
  const int d = blockIdx.x;
  const int tid = threadIdx.x, lane = tid & 63, w = tid >> 6;
  const u16* h = ssm_k + (size_t)d * S;
  const u16* u = kvT + (size_t)d * S;
  {
    const int base = 2040 - 8 * tid;
    u32x4 hi = *(const u32x4*)(h + base);
    u32x4 z = {0u, 0u, 0u, 0u};
    u32x4 lo = (base >= 8) ? *(const u32x4*)(h + base - 8) : z;
    unsigned Dw[8] = {lo[0], lo[1], lo[2], lo[3], hi[0], hi[1], hi[2], hi[3]};
    unsigned ent[16];
#pragma unroll
    for (int c = 0; c < 4; ++c) {
      ent[4 * c + 0] = __builtin_amdgcn_alignbit(Dw[7 - c], Dw[7 - c], 16);
      ent[4 * c + 1] = __builtin_amdgcn_alignbit(Dw[6 - c], Dw[6 - c], 16);
      ent[4 * c + 2] = (Dw[7 - c] & 0xffffu) | (Dw[6 - c] & 0xffff0000u);
      ent[4 * c + 3] = (Dw[6 - c] & 0xffffu) | (Dw[5 - c] & 0xffff0000u);
    }
    u32x4* tp = (u32x4*)(lds + tid * 64);
#pragma unroll
    for (int c = 0; c < 4; ++c) {
      u32x4 q = {ent[4 * c], ent[4 * c + 1], ent[4 * c + 2], ent[4 * c + 3]};
      tp[c] = q;
    }
    if (tid < 32) *(u32x4*)(lds + 2048 * 8 + tid * 16) = z;
    u32x4 uu = *(const u32x4*)(u + tid * 8);
    *(u32x4*)(lds + CV_U_OFF + (32 + (tid >> 3)) * CV_U_ROW + (tid & 7) * 16) = uu;
    for (int i = tid; i < 32 * CV_U_ROW / 16; i += 256) *(u32x4*)(lds + CV_U_OFF + i * 16) = z;
  }
  __syncthreads();
  const int r = lane & 31, hh = lane >> 5;
  f32x16 acc0 = {}, acc1 = {};
  {
    const char* tb = lds + (2047 - r + 8 * hh) * 8;
    const char* ub = lds + CV_U_OFF + (r + 32) * CV_U_ROW + hh * 16;
    auto ldT = [&](int q) -> s16x8 {
      const char* p = tb - q * 128;
      s16x4 a = *(const s16x4*)p;
      s16x4 b = *(const s16x4*)(p + 32);
      return __builtin_shufflevector(a, b, 0, 1, 2, 3, 4, 5, 6, 7);
    };
    const int m0 = 8 * w;
    s16x8 Tm3 = ldT(4 * m0 - 3), Tm2 = ldT(4 * m0 - 2);
#pragma unroll 2
    for (int mm = 0; mm < 8; ++mm) {
      const int m = m0 + mm;
      s16x8 Tm1 = ldT(4 * m - 1), T0 = ldT(4 * m), T1 = ldT(4 * m + 1), T2 = ldT(4 * m + 2);
      const char* up = ub - m * CV_U_ROW;
      s16x8 U0 = *(const s16x8*)(up), U1 = *(const s16x8*)(up + 32), U2 = *(const s16x8*)(up + 64), U3 = *(const s16x8*)(up + 96);
      acc0 = __builtin_amdgcn_mfma_f32_32x32x16_bf16(U0, T0, acc0, 0, 0, 0);
      acc1 = __builtin_amdgcn_mfma_f32_32x32x16_bf16(U0, T2, acc1, 0, 0, 0);
      acc0 = __builtin_amdgcn_mfma_f32_32x32x16_bf16(U1, Tm1, acc0, 0, 0, 0);
      acc1 = __builtin_amdgcn_mfma_f32_32x32x16_bf16(U1, T1, acc1, 0, 0, 0);
      acc0 = __builtin_amdgcn_mfma_f32_32x32x16_bf16(U2, Tm2, acc0, 0, 0, 0);
      acc1 = __builtin_amdgcn_mfma_f32_32x32x16_bf16(U2, T0, acc1, 0, 0, 0);
      acc0 = __builtin_amdgcn_mfma_f32_32x32x16_bf16(U3, Tm3, acc0, 0, 0, 0);
      acc1 = __builtin_amdgcn_mfma_f32_32x32x16_bf16(U3, Tm1, acc1, 0, 0, 0);
      Tm3 = T1; Tm2 = T2;
    }
  }
  __syncthreads();
  {
    float* P = (float*)lds + w * 2048;
#pragma unroll
    for (int reg = 0; reg < 16; ++reg) {
      const int i = (reg & 3) + 8 * (reg >> 2) + 4 * hh;
      P[64 * i + r] = acc0[reg];
      P[64 * i + 32 + r] = acc1[reg];
    }
  }
  __syncthreads();
  {
    const float* P = (const float*)lds + tid * 8;
    float s[8];
#pragma unroll
    for (int e = 0; e < 8; ++e) s[e] = 0.f;
#pragma unroll
    for (int ww = 0; ww < 4; ++ww) {
      f32x4 a = *(const f32x4*)(P + ww * 2048), b = *(const f32x4*)(P + ww * 2048 + 4);
#pragma unroll
      for (int e = 0; e < 4; ++e) { s[e] += a[e]; s[4 + e] += b[e]; }
    }
    u32x4 uu = *(const u32x4*)(lds + CV_U_OFF + (32 + (tid >> 3)) * CV_U_ROW + (tid & 7) * 16);
    const float Dv = bf2f(ssm_D[d]);
    u32x4 o;
#pragma unroll
    for (int e = 0; e < 4; ++e) {
      float y0 = rbf(s[2 * e]) + rbf(bflo(uu[e]) * Dv);
      float y1 = rbf(s[2 * e + 1]) + rbf(bfhi(uu[e]) * Dv);
      o[e] = pack2(y0, y1);
    }
    *(u32x4*)(YT + (size_t)d * S + tid * 8) = o;
  }
}

__device__ __forceinline__ float wave_sum(float v) {
#pragma unroll
  for (int o = 32; o > 0; o >>= 1) v += __shfl_xor(v, o, 64);
  return v;
}
__device__ __forceinline__ void ln_row(const float (&x)[10], float (&o)[10]) {
  float s = 0.f;
#pragma unroll
  for (int i = 0; i < 10; ++i) s += x[i];
  const float mean = wave_sum(s) * (1.0f / D);
  float q = 0.f;
#pragma unroll
  for (int i = 0; i < 10; ++i) { float dd = x[i] - mean; q += dd * dd; }
  const float var = rbf(wave_sum(q) * (1.0f / D));
  const float mb = rbf(mean);
  const float sd = rbf(sqrtf(rbf(var + 1e-5f)));
#pragma unroll
  for (int i = 0; i < 10; ++i) o[i] = rbf(rbf(x[i] - mb) / sd);
}

template <bool FIRST>
__global__ __launch_bounds__(256) void k_row(const u16* __restrict__ xin, const u16* __restrict__ YT,
                                             u16* __restrict__ Y, u16* __restrict__ xout, u16* __restrict__ xn) {
  __shared__ __attribute__((aligned(16))) u16 yt[8][D + 8];
  const int t0 = blockIdx.x * 8;
  const int tid = threadIdx.x, lane = tid & 63, w = tid >> 6;
  if (FIRST) {
    for (int dd = tid; dd < D; dd += 256) {
      u32x4 q = *(const u32x4*)(YT + (size_t)dd * S + t0);
#pragma unroll
      for (int e = 0; e < 4; ++e) {
        yt[2 * e][dd] = (u16)(q[e] & 0xffffu);
        yt[2 * e + 1][dd] = (u16)(q[e] >> 16);
      }
    }
    __syncthreads();
  }
#pragma unroll
  for (int rr = 0; rr < 2; ++rr) {
    const int tl = w * 2 + rr, t = t0 + tl;
    float x[10], y[10], n[10];
#pragma unroll
    for (int j = 0; j < 5; ++j) {
      const int c = 2 * lane + 128 * j;
      unsigned p = *(const unsigned*)(xin + (size_t)t * D + c);
      x[2 * j] = bflo(p); x[2 * j + 1] = bfhi(p);
      unsigned py;
      if (FIRST) { py = *(const unsigned*)&yt[tl][c]; *(unsigned*)(Y + (size_t)t * D + c) = py; }
      else py = *(const unsigned*)(Y + (size_t)t * D + c);
      y[2 * j] = bflo(py); y[2 * j + 1] = bfhi(py);
    }
    ln_row(x, n);
#pragma unroll
    for (int i = 0; i < 10; ++i) x[i] = rbf(x[i] + rbf(n[i] * y[i]));
    ln_row(x, n);
#pragma unroll
    for (int j = 0; j < 5; ++j) {
      const int c = 2 * lane + 128 * j;
      *(unsigned*)(xout + (size_t)t * D + c) = pack2(x[2 * j], x[2 * j + 1]);
      *(unsigned*)(xn + (size_t)t * D + c) = pack2(n[2 * j], n[2 * j + 1]);
    }
  }
}

__device__ __forceinline__ void glds16(const void* g, void* l) {
  __builtin_amdgcn_global_load_lds((const unsigned*)g, (unsigned*)l, 16, 0, 0);
}
__device__ __forceinline__ s16x8 tr_frag(const char* p0, const char* p1) {
  s16x4 a = __builtin_amdgcn_ds_read_tr16_b64_v4i16((lds_s16x4_ptr)p0);
  s16x4 b = __builtin_amdgcn_ds_read_tr16_b64_v4i16((lds_s16x4_ptr)p1);
  return __builtin_shufflevector(a, b, 0, 1, 2, 3, 4, 5, 6, 7);
}

constexpr int G1_A = 256 * 64 * 2, G1_B = 64 * 128 * 2, G1_ST = G1_A + G1_B, G1_NS = 3;

__global__ __launch_bounds__(512) void k_gemm1(const u16* __restrict__ xn, const u16* __restrict__ wg,
                                               const u16* __restrict__ wu, u16* __restrict__ hout) {
  __shared__ __attribute__((aligned(1024))) char lds[G1_NS * G1_ST];
  const int id = blockIdx.x;
  const int nt = (id & 7) + 8 * (id >> 6), mt = (id >> 3) & 7;
  if (nt >= 27) return;
  const int m0 = mt * 256, f0 = nt * 64;
  const int tid = threadIdx.x, lane = tid & 63, w = tid >> 6;
  const int wm = w >> 1, wn = w & 1;
  const u16* asrc[4];
#pragma unroll
  for (int i = 0; i < 4; ++i) {
    const int row = (w * 4 + i) * 8 + (lane >> 3);
    const int ch = (lane & 7) ^ ((row >> 1) & 7);
    asrc[i] = xn + (size_t)(m0 + row) * D + ch * 8;
  }
  const u16* bsrc[2];
#pragma unroll
  for (int i = 0; i < 2; ++i) {
    const int row = (w * 2 + i) * 4 + (lane >> 4);
    const int seg = ((lane >> 2) & 3) ^ (row & 3);
    const u16* base = (seg & 2) ? wu : wg;
    bsrc[i] = base + (size_t)row * F + f0 + (seg & 1) * 32 + (lane & 3) * 8;
  }
  auto stage = [&](int buf, int kt) {
    char* a = lds + buf * G1_ST;
    char* b = a + G1_A;
#pragma unroll
    for (int i = 0; i < 4; ++i) glds16(asrc[i] + kt * 64, a + (w * 4 + i) * 1024);
#pragma unroll
    for (int i = 0; i < 2; ++i) glds16(bsrc[i] + (size_t)kt * 64 * F, b + (w * 2 + i) * 1024);
  };
  f32x16 ag[2] = {}, au[2] = {};
  const int r = lane & 31, hh = lane >> 5;
  int aoff[2];
#pragma unroll
  for (int mi = 0; mi < 2; ++mi) aoff[mi] = (wm * 64 + mi * 32 + r) * 128;
  const int asw = (r >> 1) & 7;
  const int blk = (lane >> 4) & 1, q = (lane & 15) >> 2, p = lane & 3;
  const int boff_g = (8 * hh + q) * 256 + ((wn ^ q) * 64) + blk * 32 + p * 8;
  const int boff_u = (8 * hh + q) * 256 + (((2 + wn) ^ q) * 64) + blk * 32 + p * 8;

  constexpr int NT = D / 64;
  stage(0, 0);
  stage(1, 1);
  for (int kt = 0; kt < NT; ++kt) {
    if (kt + 1 < NT) WAIT_V(6); else WAIT_V(0);
    WAIT_L(0);
    __builtin_amdgcn_s_barrier();
    if (kt + 2 < NT) stage((kt + 2) % 3, kt + 2);
    const char* a = lds + (kt % 3) * G1_ST;
    const char* b = a + G1_A;
#pragma unroll
    for (int ks = 0; ks < 4; ++ks) {
      s16x8 af[2];
#pragma unroll
      for (int mi = 0; mi < 2; ++mi) af[mi] = *(const s16x8*)(a + aoff[mi] + (((2 * ks + hh) ^ asw) * 16));
      s16x8 bg = tr_frag(b + boff_g + ks * 4096, b + boff_g + ks * 4096 + 1024);
      s16x8 bu = tr_frag(b + boff_u + ks * 4096, b + boff_u + ks * 4096 + 1024);
#pragma unroll
      for (int mi = 0; mi < 2; ++mi) {
        ag[mi] = __builtin_amdgcn_mfma_f32_32x32x16_bf16(af[mi], bg, ag[mi], 0, 0, 0);
        au[mi] = __builtin_amdgcn_mfma_f32_32x32x16_bf16(af[mi], bu, au[mi], 0, 0, 0);
      }
    }
  }
#pragma unroll
  for (int mi = 0; mi < 2; ++mi) {
#pragma unroll
    for (int reg = 0; reg < 16; ++reg) {
      const int row = m0 + wm * 64 + mi * 32 + (reg & 3) + 8 * (reg >> 2) + 4 * hh;
      const float g = rbf(ag[mi][reg]), uu = rbf(au[mi][reg]);
      const float sg = rbf(1.0f / (1.0f + __expf(-g)));
      hout[(size_t)row * F + f0 + wn * 32 + r] = f2bf(sg * uu);
    }
  }
}

constexpr int G2_A = 128 * 64 * 2, G2_B = 64 * 64 * 2, G2_ST = G2_A + G2_B, G2_NS = 4;

__global__ __launch_bounds__(256) void k_gemm2(const u16* __restrict__ hin, const u16* __restrict__ wd,
                                               const u16* __restrict__ xres, u16* __restrict__ out) {
  __shared__ __attribute__((aligned(1024))) char lds[G2_NS * G2_ST];
  const int id = blockIdx.x;
  const int mt = id & 15, nt = id >> 4;
  const int m0 = mt * 128, n0 = nt * 64;
  const int tid = threadIdx.x, lane = tid & 63, w = tid >> 6;
  const int wm = w >> 1, wn = w & 1;
  const u16* asrc[4];
#pragma unroll
  for (int i = 0; i < 4; ++i) {
    const int row = (w * 4 + i) * 8 + (lane >> 3);
    const int ch = (lane & 7) ^ ((row >> 1) & 7);
    asrc[i] = hin + (size_t)(m0 + row) * F + ch * 8;
  }
  const u16* bsrc[2];
#pragma unroll
  for (int i = 0; i < 2; ++i) {
    const int row = (w * 2 + i) * 8 + (lane >> 3);
    const int seg = ((lane >> 2) & 1) ^ ((row >> 1) & 1);
    bsrc[i] = wd + (size_t)row * D + n0 + seg * 32 + (lane & 3) * 8;
  }
  auto stage = [&](int buf, int kt) {
    char* a = lds + buf * G2_ST;
    char* b = a + G2_A;
#pragma unroll
    for (int i = 0; i < 4; ++i) glds16(asrc[i] + kt * 64, a + (w * 4 + i) * 1024);
#pragma unroll
    for (int i = 0; i < 2; ++i) glds16(bsrc[i] + (size_t)kt * 64 * D, b + (w * 2 + i) * 1024);
  };
  f32x16 acc[2] = {};
  const int r = lane & 31, hh = lane >> 5;
  int aoff[2];
#pragma unroll
  for (int mi = 0; mi < 2; ++mi) aoff[mi] = (wm * 64 + mi * 32 + r) * 128;
  const int asw = (r >> 1) & 7;
  const int blk = (lane >> 4) & 1, q = (lane & 15) >> 2, p = lane & 3;
  const int boff = (8 * hh + q) * 128 + ((wn ^ ((q >> 1) & 1)) * 64) + blk * 32 + p * 8;

  constexpr int NT = F / 64;
  stage(0, 0);
  stage(1, 1);
  stage(2, 2);
  for (int kt = 0; kt < NT; ++kt) {
    if (kt + 2 < NT) WAIT_V(12); else if (kt + 1 < NT) WAIT_V(6); else WAIT_V(0);
    WAIT_L(0);
    __builtin_amdgcn_s_barrier();
    if (kt + 3 < NT) stage((kt + 3) & 3, kt + 3);
    const char* a = lds + (kt & 3) * G2_ST;
    const char* b = a + G2_A;
#pragma unroll
    for (int ks = 0; ks < 4; ++ks) {
      s16x8 af[2];
#pragma unroll
      for (int mi = 0; mi < 2; ++mi) af[mi] = *(const s16x8*)(a + aoff[mi] + (((2 * ks + hh) ^ asw) * 16));
      s16x8 bf = tr_frag(b + boff + ks * 2048, b + boff + ks * 2048 + 512);
#pragma unroll
      for (int mi = 0; mi < 2; ++mi) acc[mi] = __builtin_amdgcn_mfma_f32_32x32x16_bf16(af[mi], bf, acc[mi], 0, 0, 0);
    }
  }
#pragma unroll
  for (int mi = 0; mi < 2; ++mi) {
#pragma unroll
    for (int reg = 0; reg < 16; ++reg) {
      const int row = m0 + wm * 64 + mi * 32 + (reg & 3) + 8 * (reg >> 2) + 4 * hh;
      const size_t idx = (size_t)row * D + n0 + wn * 32 + r;
      out[idx] = f2bf(bf2f(xres[idx]) + rbf(acc[mi][reg]));
    }
  }
}

extern "C" void kernel_launch(void* const* d_in, const int* in_sizes, int n_in, void* d_out, int out_size,
                              void* d_ws, size_t ws_size, hipStream_t stream) {
  const u16* q = (const u16*)d_in[0];
  const u16* k = (const u16*)d_in[1];
  const u16* v = (const u16*)d_in[2];
  const u16* shift_k = (const u16*)d_in[3];
  const u16* ssm_k = (const u16*)d_in[4];
  const u16* ssm_D = (const u16*)d_in[5];
  const u16* wg = (const u16*)d_in[7];
  const u16* wu = (const u16*)d_in[8];
  const u16* wd = (const u16*)d_in[9];
  u16* out = (u16*)d_out;
  char* ws = (char*)d_ws;
  constexpr size_t SZ = (size_t)S * D * 2;
  u16* kvT = (u16*)(ws + 0 * SZ);
  u16* YT = (u16*)(ws + 1 * SZ);
  u16* Y = (u16*)(ws + 2 * SZ);
  u16* x1 = (u16*)(ws + 3 * SZ);
  u16* xn = (u16*)(ws + 4 * SZ);
  u16* x2 = (u16*)(ws + 5 * SZ);
  u16* x3 = (u16*)(ws + 6 * SZ);
  u16* h = (u16*)(ws + 7 * SZ);

  k_prep<<<dim3(S / 64, D / 64), 256, 0, stream>>>(k, v, shift_k, kvT);
  k_conv<<<D, 256, 0, stream>>>(kvT, ssm_k, ssm_D, YT);
  k_row<true><<<S / 8, 256, 0, stream>>>(q, YT, Y, x1, xn);
  k_gemm1<<<256, 512, 0, stream>>>(xn, wg, wu, h);
  k_gemm2<<<160, 256, 0, stream>>>(h, wd, x1, x2);
  k_row<false><<<S / 8, 256, 0, stream>>>(x2, YT, Y, x3, xn);
  k_gemm1<<<256, 512, 0, stream>>>(xn, wg, wu, h);
  k_gemm2<<<160, 256, 0, stream>>>(h, wd, x3, out);
}
```

```cpp
#include <hip/hip_runtime.h>
#include <hip/hip_bf16.h>

typedef unsigned short u16;
typedef __attribute__((ext_vector_type(4))) short s16x4;
typedef __attribute__((ext_vector_type(8))) short s16x8;
typedef __attribute__((ext_vector_type(16))) float f32x16;
typedef __attribute__((ext_vector_type(4))) float f32x4;
typedef __attribute__((ext_vector_type(4))) unsigned u32x4;
typedef __attribute__((ext_vector_type(2))) unsigned u32x2;
typedef __attribute__((address_space(3))) s16x4* lds_s16x4_ptr;

constexpr int S = 2048, D = 640, F = 1728;

__device__ __forceinline__ float bf2f(u16 b) { return __uint_as_float((unsigned)b << 16); }
__device__ __forceinline__ float bflo(unsigned p) { return __uint_as_float(p << 16); }
__device__ __forceinline__ float bfhi(unsigned p) { return __uint_as_float(p & 0xffff0000u); }
__device__ __forceinline__ u16 f2bf(float x) { __bf16 b = (__bf16)x; return __builtin_bit_cast(u16, b); }
__device__ __forceinline__ float rbf(float x) { return bf2f(f2bf(x)); }
__device__ __forceinline__ unsigned pack2(float lo, float hi) { return (unsigned)f2bf(lo) | ((unsigned)f2bf(hi) << 16); }

#define WAIT_V(n) asm volatile("s_waitcnt vmcnt(%0)" ::"n"(n) : "memory")
#define WAIT_L(n) asm volatile("s_waitcnt lgkmcnt(%0)" ::"n"(n) : "memory")

__global__ __launch_bounds__(256) void k_prep(const u16* __restrict__ k, const u16* __restrict__ v,
                                              const u16* __restrict__ shift_k, u16* __restrict__ kvT) {
  __shared__ __attribute__((aligned(16))) u16 tile[64][72];
  const int t0 = blockIdx.x * 64, d0 = blockIdx.y * 64;
  const int tid = threadIdx.x;
  const int cd = (tid & 7) * 8, rt = tid >> 3;
  float w[8][4];
  {
    const u32x4* sp = (const u32x4*)(shift_k + (size_t)(d0 + cd) * 4);
#pragma unroll
    for (int i = 0; i < 4; ++i) {
      u32x4 q = sp[i];
      w[2 * i][0] = bflo(q[0]); w[2 * i][1] = bfhi(q[0]); w[2 * i][2] = bflo(q[1]); w[2 * i][3] = bfhi(q[1]);
      w[2 * i + 1][0] = bflo(q[2]); w[2 * i + 1][1] = bfhi(q[2]); w[2 * i + 1][2] = bflo(q[3]); w[2 * i + 1][3] = bfhi(q[3]);
    }
  }
#pragma unroll
  for (int pass = 0; pass < 2; ++pass) {
    const int tl = rt + 32 * pass, t = t0 + tl;
    u32x4 kk[4];
#pragma unroll
    for (int j = 0; j < 4; ++j) {
      u32x4 z = {0u, 0u, 0u, 0u};
      kk[j] = (t - j >= 0) ? *(const u32x4*)(k + (size_t)(t - j) * D + d0 + cd) : z;
    }
    u32x4 vv = *(const u32x4*)(v + (size_t)t * D + d0 + cd);
#pragma unroll
    for (int e = 0; e < 8; ++e) {
      float ks = 0.f;
#pragma unroll
      for (int j = 3; j >= 0; --j) {
        unsigned p = kk[j][e >> 1];
        float kf = (e & 1) ? bfhi(p) : bflo(p);
        ks = rbf(ks + rbf(kf * w[e][j]));
      }
      unsigned p = vv[e >> 1];
      float vf = (e & 1) ? bfhi(p) : bflo(p);
      tile[cd + e][tl] = f2bf(ks * vf);
    }
  }
  __syncthreads();
  {
    const int dl = tid >> 2, c = (tid & 3) * 16;
    u32x4 a = *(const u32x4*)&tile[dl][c];
    u32x4 b = *(const u32x4*)&tile[dl][c + 8];
    u16* dst = kvT + (size_t)(d0 + dl) * S + t0 + c;
    *(u32x4*)dst = a;
    *(u32x4*)(dst + 8) = b;
  }
}

constexpr int CV_T_BYTES = 2112 * 8;
constexpr int CV_U_OFF = 32768;
constexpr int CV_U_ROW = 144;
constexpr int CV_LDS = CV_U_OFF + 64 * CV_U_ROW;

constexpr int PK1_CHUNKS = 27 * 10 * 2 * 64 * 8, PK2_CHUNKS = 8 * 27 * 64 * 10;
constexpr int PK_WGS = (PK1_CHUNKS + PK2_CHUNKS) / 1024;
__device__ __forceinline__ void pack_weights(int wgi, const u16* __restrict__ wg, const u16* __restrict__ wu,
                                             const u16* __restrict__ wd, u16* __restrict__ wpk1, u16* __restrict__ wpk2) {
  u32x4 v[4];
#pragma unroll
  for (int i = 0; i < 4; ++i) {
    const int c = wgi * 1024 + i * 256 + threadIdx.x;
    if (c < PK1_CHUNKS) {
      const int cp = c & 7, k = (c >> 3) & 63, gu = (c >> 9) & 1, tk = c >> 10;
      const int nt = tk / 10, kt = tk - nt * 10;
      const int col = nt * 64 + (((cp >> 2) ^ ((k >> 1) & 1)) * 32) + (cp & 3) * 8;
      v[i] = *(const u32x4*)((gu ? wu : wg) + (size_t)(kt * 64 + k) * F + col);
    } else {
      const int c2 = c - PK1_CHUNKS;
      const int rk = c2 / 10, ch = c2 - rk * 10;
      const int tk = rk >> 6, k = rk & 63;
      const int nt = tk / 27, kt = tk - nt * 27;
      v[i] = *(const u32x4*)(wd + (size_t)(kt * 64 + k) * D + nt * 80 + ch * 8);
    }
  }
#pragma unroll
  for (int i = 0; i < 4; ++i) {
    const int c = wgi * 1024 + i * 256 + threadIdx.x;
    if (c < PK1_CHUNKS) *(u32x4*)(wpk1 + (size_t)c * 8) = v[i];
    else *(u32x4*)(wpk2 + (size_t)(c - PK1_CHUNKS) * 8) = v[i];
  }
}

__global__ __launch_bounds__(256) void k_conv(const u16* __restrict__ kvT, const u16* __restrict__ ssm_k,
                                              const u16* __restrict__ ssm_D, u16* __restrict__ YT,
                                              const u16* __restrict__ wg, const u16* __restrict__ wu,
                                              const u16* __restrict__ wd, u16* __restrict__ wpk1, u16* __restrict__ wpk2) {
  __shared__ __attribute__((aligned(16))) char lds[CV_LDS];
  if (blockIdx.x >= D) { pack_weights(blockIdx.x - D, wg, wu, wd, wpk1, wpk2); return; }
  const int d = blockIdx.x;
  const int tid = threadIdx.x, lane = tid & 63, w = tid >> 6;
  const u16* h = ssm_k + (size_t)d * S;
  const u16* u = kvT + (size_t)d * S;
  {
    const int base = 2040 - 8 * tid;
    u32x4 hi = *(const u32x4*)(h + base);
    u32x4 z = {0u, 0u, 0u, 0u};
    u32x4 lo = (base >= 8) ? *(const u32x4*)(h + base - 8) : z;
    unsigned Dw[8] = {lo[0], lo[1], lo[2], lo[3], hi[0], hi[1], hi[2], hi[3]};
    unsigned ent[16];
#pragma unroll
    for (int c = 0; c < 4; ++c) {
      ent[4 * c + 0] = __builtin_amdgcn_alignbit(Dw[7 - c], Dw[7 - c], 16);
      ent[4 * c + 1] = __builtin_amdgcn_alignbit(Dw[6 - c], Dw[6 - c], 16);
      ent[4 * c + 2] = (Dw[7 - c] & 0xffffu) | (Dw[6 - c] & 0xffff0000u);
      ent[4 * c + 3] = (Dw[6 - c] & 0xffffu) | (Dw[5 - c] & 0xffff0000u);
    }
    u32x4* tp = (u32x4*)(lds + tid * 64);
#pragma unroll
    for (int c = 0; c < 4; ++c) {
      u32x4 q = {ent[4 * c], ent[4 * c + 1], ent[4 * c + 2], ent[4 * c + 3]};
      tp[c] = q;
    }
    if (tid < 32) *(u32x4*)(lds + 2048 * 8 + tid * 16) = z;
    u32x4 uu = *(const u32x4*)(u + tid * 8);
    *(u32x4*)(lds + CV_U_OFF + (32 + (tid >> 3)) * CV_U_ROW + (tid & 7) * 16) = uu;
    for (int i = tid; i < 32 * CV_U_ROW / 16; i += 256) *(u32x4*)(lds + CV_U_OFF + i * 16) = z;
  }
  __syncthreads();
  const int r = lane & 31, hh = lane >> 5;
  f32x16 acc0 = {}, acc1 = {};
  {
    const char* tb = lds + (2047 - r + 8 * hh) * 8;
    const char* ub = lds + CV_U_OFF + (r + 32) * CV_U_ROW + hh * 16;
    auto ldT = [&](int q) -> s16x8 {
      const char* p = tb - q * 128;
      s16x4 a = *(const s16x4*)p;
      s16x4 b = *(const s16x4*)(p + 32);
      return __builtin_shufflevector(a, b, 0, 1, 2, 3, 4, 5, 6, 7);
    };
    const int m0 = 8 * w;
    s16x8 Tm3 = ldT(4 * m0 - 3), Tm2 = ldT(4 * m0 - 2);
#pragma unroll 2
    for (int mm = 0; mm < 8; ++mm) {
      const int m = m0 + mm;
      s16x8 Tm1 = ldT(4 * m - 1), T0 = ldT(4 * m), T1 = ldT(4 * m + 1), T2 = ldT(4 * m + 2);
      const char* up = ub - m * CV_U_ROW;
      s16x8 U0 = *(const s16x8*)(up), U1 = *(const s16x8*)(up + 32), U2 = *(const s16x8*)(up + 64), U3 = *(const s16x8*)(up + 96);
      acc0 = __builtin_amdgcn_mfma_f32_32x32x16_bf16(U0, T0, acc0, 0, 0, 0);
      acc1 = __builtin_amdgcn_mfma_f32_32x32x16_bf16(U0, T2, acc1, 0, 0, 0);
      acc0 = __builtin_amdgcn_mfma_f32_32x32x16_bf16(U1, Tm1, acc0, 0, 0, 0);
      acc1 = __builtin_amdgcn_mfma_f32_32x32x16_bf16(U1, T1, acc1, 0, 0, 0);
      acc0 = __builtin_amdgcn_mfma_f32_32x32x16_bf16(U2, Tm2, acc0, 0, 0, 0);
      acc1 = __builtin_amdgcn_mfma_f32_32x32x16_bf16(U2, T0, acc1, 0, 0, 0);
      acc0 = __builtin_amdgcn_mfma_f32_32x32x16_bf16(U3, Tm3, acc0, 0, 0, 0);
      acc1 = __builtin_amdgcn_mfma_f32_32x32x16_bf16(U3, Tm1, acc1, 0, 0, 0);
      Tm3 = T1; Tm2 = T2;
    }
  }
  asm volatile("" :: "v"(acc0[0]), "v"(acc1[0]));
  __syncthreads();
  {
    float* P = (float*)lds + w * 2048;
#pragma unroll
    for (int reg = 0; reg < 16; ++reg) {
      const int i = (reg & 3) + 8 * (reg >> 2) + 4 * hh;
      P[64 * i + r] = acc0[reg];
      P[64 * i + 32 + r] = acc1[reg];
    }
  }
  __syncthreads();
  {
    const float* P = (const float*)lds + tid * 8;
    float s[8];
#pragma unroll
    for (int e = 0; e < 8; ++e) s[e] = 0.f;
#pragma unroll
    for (int ww = 0; ww < 4; ++ww) {
      f32x4 a = *(const f32x4*)(P + ww * 2048), b = *(const f32x4*)(P + ww * 2048 + 4);
#pragma unroll
      for (int e = 0; e < 4; ++e) { s[e] += a[e]; s[4 + e] += b[e]; }
    }
    u32x4 uu = *(const u32x4*)(lds + CV_U_OFF + (32 + (tid >> 3)) * CV_U_ROW + (tid & 7) * 16);
    const float Dv = bf2f(ssm_D[d]);
    u32x4 o;
#pragma unroll
    for (int e = 0; e < 4; ++e) {
      float y0 = rbf(s[2 * e]) + rbf(bflo(uu[e]) * Dv);
      float y1 = rbf(s[2 * e + 1]) + rbf(bfhi(uu[e]) * Dv);
      o[e] = pack2(y0, y1);
    }
    *(u32x4*)(YT + (size_t)d * S + tid * 8) = o;
  }
}

template <int CTRL> __device__ __forceinline__ float dpp_add(float v) {
  return v + __builtin_bit_cast(float, __builtin_amdgcn_update_dpp(0, __builtin_bit_cast(int, v), CTRL, 0xf, 0xf, true));
}
__device__ __forceinline__ float wave_sum(float v) {
  v = dpp_add<0xB1>(v);
  v = dpp_add<0x4E>(v);
  v = dpp_add<0x141>(v);
  v = dpp_add<0x140>(v);
  const int iv = __builtin_bit_cast(int, v);
  const float a = __builtin_bit_cast(float, __builtin_amdgcn_readlane(iv, 0));
  const float b = __builtin_bit_cast(float, __builtin_amdgcn_readlane(iv, 16));
  const float c = __builtin_bit_cast(float, __builtin_amdgcn_readlane(iv, 32));
  const float d = __builtin_bit_cast(float, __builtin_amdgcn_readlane(iv, 48));
  return (a + b) + (c + d);
}
__device__ __forceinline__ void ln_row(const float (&x)[10], float (&o)[10]) {
  float s = 0.f;
#pragma unroll
  for (int i = 0; i < 10; ++i) s += x[i];
  const float mean = wave_sum(s) * (1.0f / D);
  float q = 0.f;
#pragma unroll
  for (int i = 0; i < 10; ++i) { float dd = x[i] - mean; q += dd * dd; }
  const float var = rbf(wave_sum(q) * (1.0f / D));
  const float mb = rbf(mean);
  const float sd = rbf(sqrtf(rbf(var + 1e-5f)));
  const float rs = 1.0f / sd;
#pragma unroll
  for (int i = 0; i < 10; ++i) o[i] = rbf(rbf(x[i] - mb) * rs);
}
__device__ __forceinline__ void unpack10(const u32x4 a, const unsigned b, float (&x)[10]) {
#pragma unroll
  for (int e = 0; e < 4; ++e) { x[2 * e] = bflo(a[e]); x[2 * e + 1] = bfhi(a[e]); }
  x[8] = bflo(b); x[9] = bfhi(b);
}
__device__ __forceinline__ void store10(u16* rowp, int lane, const float (&x)[10]) {
  u32x4 a;
#pragma unroll
  for (int e = 0; e < 4; ++e) a[e] = pack2(x[2 * e], x[2 * e + 1]);
  *(u32x4*)(rowp + 8 * lane) = a;
  *(unsigned*)(rowp + 512 + 2 * lane) = pack2(x[8], x[9]);
}

template <int MODE>
__global__ __launch_bounds__(512) void k_row(const u16* __restrict__ xin, const float* __restrict__ part,
                                             const u16* __restrict__ YT, u16* __restrict__ Y,
                                             u16* __restrict__ xout, u16* __restrict__ xn) {
  __shared__ __attribute__((aligned(16))) u16 yt[8][D + 8];
  const int t0 = (blockIdx.x & 7) * 256 + (blockIdx.x >> 3) * 8;
  const int tid = threadIdx.x, lane = tid & 63, w = tid >> 6;
  const int t = t0 + w;
  float x[10], y[10], n[10];
  {
    const u16* rp = xin + (size_t)t * D;
    unpack10(*(const u32x4*)(rp + 8 * lane), *(const unsigned*)(rp + 512 + 2 * lane), x);
  }
  if (MODE == 0) {
    for (int dd = tid; dd < D; dd += 512) {
      u32x4 q = *(const u32x4*)(YT + (size_t)dd * S + t0);
#pragma unroll
      for (int e = 0; e < 4; ++e) {
        yt[2 * e][dd] = (u16)(q[e] & 0xffffu);
        yt[2 * e + 1][dd] = (u16)(q[e] >> 16);
      }
    }
    __syncthreads();
    u32x4 a = *(const u32x4*)&yt[w][8 * lane];
    unsigned b = *(const unsigned*)&yt[w][512 + 2 * lane];
    unpack10(a, b, y);
    *(u32x4*)(Y + (size_t)t * D + 8 * lane) = a;
    *(unsigned*)(Y + (size_t)t * D + 512 + 2 * lane) = b;
  } else {
    const u16* rp = Y + (size_t)t * D;
    unpack10(*(const u32x4*)(rp + 8 * lane), *(const unsigned*)(rp + 512 + 2 * lane), y);
  }
  asm volatile("" :: "v"(x[0]), "v"(y[0]), "v"(x[9]), "v"(y[9]));
  ln_row(x, n);
#pragma unroll
  for (int i = 0; i < 10; ++i) x[i] = rbf(x[i] + rbf(n[i] * y[i]));
  ln_row(x, n);
  asm volatile("" :: "v"(n[0]), "v"(n[9]));
  store10(xout + (size_t)t * D, lane, x);
  {
    const int sw = (t >> 1) & 7;
    u16* bp = xn + (size_t)(t >> 3) * 512 + (t & 7) * 64;
    u32x4 a;
#pragma unroll
    for (int e = 0; e < 4; ++e) a[e] = pack2(n[2 * e], n[2 * e + 1]);
    *(u32x4*)(bp + (size_t)(lane >> 3) * (256 * 512) + (((lane & 7) ^ sw) * 8)) = a;
    *(unsigned*)(bp + (size_t)(8 + (lane >> 5)) * (256 * 512) + ((((lane & 31) >> 2) ^ sw) * 8) + 2 * (lane & 3)) = pack2(n[8], n[9]);
  }
}

__device__ __forceinline__ unsigned lds_addr(const void* p) {
  return (unsigned)(uintptr_t)(const __attribute__((address_space(3))) char*)p;
}
__device__ __forceinline__ void glds16(const void* sbase, unsigned voff, unsigned ldst) {
  unsigned keep;
  asm volatile("s_mov_b32 %0, m0\n\ts_mov_b32 m0, %3\n\ts_nop 0\n\tglobal_load_lds_dwordx4 %1, %2\n\ts_mov_b32 m0, %0"
               : "=&s"(keep) : "v"(voff), "s"(sbase), "s"(ldst) : "memory");
}
__device__ __forceinline__ s16x8 tr_frag(const char* p0, const char* p1) {
  s16x4 a = __builtin_amdgcn_ds_read_tr16_b64_v4i16((lds_s16x4_ptr)p0);
  s16x4 b = __builtin_amdgcn_ds_read_tr16_b64_v4i16((lds_s16x4_ptr)p1);
  return __builtin_shufflevector(a, b, 0, 1, 2, 3, 4, 5, 6, 7);
}

#define SCHED() __builtin_amdgcn_sched_barrier(0)
#define ILV_MFMA_DS()                                        \
  do {                                                       \
    __builtin_amdgcn_sched_group_barrier(0x008, 1, 0);       \
    __builtin_amdgcn_sched_group_barrier(0x100, 2, 0);       \
    __builtin_amdgcn_sched_group_barrier(0x008, 1, 0);       \
    __builtin_amdgcn_sched_group_barrier(0x100, 2, 0);       \
    __builtin_amdgcn_sched_group_barrier(0x008, 1, 0);       \
    __builtin_amdgcn_sched_group_barrier(0x100, 2, 0);       \
    __builtin_amdgcn_sched_group_barrier(0x008, 1, 0);       \
  } while (0)
constexpr int G1_A = 256 * 64 * 2, G1_B = 64 * 128 * 2, G1_ST = G1_A + G1_B, G1_NS = 3;

__global__ __launch_bounds__(768) void k_gemm1(const u16* __restrict__ xn, const u16* __restrict__ wpk,
                                               u16* __restrict__ hout) {
  __shared__ __attribute__((aligned(1024))) char lds[G1_NS * G1_ST];
  const int id = blockIdx.x;
  const int g = id & 7, j = id >> 3;
  const int mt = (g & 1) * 4 + (j & 3);
  const int nq = g >> 1, nj = j >> 2;
  const int nt = nq * 7 + nj;
  if (nj >= 7 || nt >= 27) return;
  const int m0 = mt * 256, f0 = nt * 64;
  const int tid = threadIdx.x, lane = tid & 63;
  const int w = __builtin_amdgcn_readfirstlane(tid >> 6);
  constexpr int NT = D / 64;
  if (w >= 8) {
    const int lw = w - 8;
    const unsigned lbase = lds_addr(lds);
    const unsigned voff = lane * 16;
    const char* abase = (const char*)xn + (size_t)(mt * 32 + lw * 8) * 1024;
    const char* bbase = (const char*)wpk + (size_t)nt * 10 * 16384 + lw * 4096;
    auto stage = [&](int buf, int kt) {
      const unsigned a = lbase + buf * G1_ST;
      const char* ab = abase + (size_t)kt * 256 * 1024;
      const char* bb = bbase + kt * 16384;
#pragma unroll
      for (int i = 0; i < 4; ++i) glds16(bb + i * 1024, voff, a + G1_A + (lw * 4 + i) * 1024);
#pragma unroll
      for (int i = 0; i < 8; ++i) glds16(ab + i * 1024, voff, a + (lw * 8 + i) * 1024);
    };
    stage(0, 0);
    stage(1, 1);
    stage(2, 2);
    WAIT_V(24);
    __builtin_amdgcn_s_barrier();
    int buf = 0;
#pragma unroll 1
    for (int kt = 0; kt + 1 < NT; ++kt) {
      if (kt + 2 < NT) WAIT_V(12); else WAIT_V(0);
      __builtin_amdgcn_s_barrier();
      if (kt + 3 < NT) stage(buf, kt + 3);
      buf = (buf == 2) ? 0 : buf + 1;
    }
    return;
  }
  const int wm = w >> 1, wn = w & 1;
  f32x16 ag[2] = {}, au[2] = {};
  const int r = lane & 31, hh = lane >> 5;
  const int arow = (wm * 64 + r) * 128;
  const int asw = (r >> 1) & 7;
  const int blk = (lane >> 4) & 1, q = (lane & 15) >> 2, p = lane & 3;
  const int boff = G1_A + (8 * hh + q) * 128 + ((wn ^ ((q >> 1) & 1)) * 64) + blk * 32 + p * 8;
  struct Frag { s16x8 a0, a1, bg, bu; };
  auto ldfrag = [&](const char* base, int ks) -> Frag {
    Frag f;
    const char* ap = base + arow + (((2 * ks + hh) ^ asw) * 16);
    const char* bp = base + boff + ks * 2048;
    f.bg = tr_frag(bp, bp + 512);
    f.a0 = *(const s16x8*)ap;
    f.bu = tr_frag(bp + 8192, bp + 8192 + 512);
    f.a1 = *(const s16x8*)(ap + 4096);
    return f;
  };
  auto mma = [&](const Frag& f) {
    ag[0] = __builtin_amdgcn_mfma_f32_32x32x16_bf16(f.bg, f.a0, ag[0], 0, 0, 0);
    au[0] = __builtin_amdgcn_mfma_f32_32x32x16_bf16(f.bu, f.a0, au[0], 0, 0, 0);
    ag[1] = __builtin_amdgcn_mfma_f32_32x32x16_bf16(f.bg, f.a1, ag[1], 0, 0, 0);
    au[1] = __builtin_amdgcn_mfma_f32_32x32x16_bf16(f.bu, f.a1, au[1], 0, 0, 0);
  };
  __builtin_amdgcn_s_barrier();
  Frag f0_ = ldfrag(lds, 0);
  int buf = 0;
#pragma unroll 1
  for (int kt = 0; kt < NT - 1; ++kt) {
    const char* base = lds + buf * G1_ST;
    SCHED();
    Frag f1_ = ldfrag(base, 1);
    mma(f0_);
    ILV_MFMA_DS();
    SCHED();
    Frag f2_ = ldfrag(base, 2);
    mma(f1_);
    ILV_MFMA_DS();
    SCHED();
    Frag f3_ = ldfrag(base, 3);
    mma(f2_);
    ILV_MFMA_DS();
    SCHED();
    buf = (buf == 2) ? 0 : buf + 1;
    WAIT_L(0);
    __builtin_amdgcn_s_barrier();
    SCHED();
    f0_ = ldfrag(lds + buf * G1_ST, 0);
    mma(f3_);
    ILV_MFMA_DS();
    SCHED();
  }
  {
    const char* base = lds + buf * G1_ST;
    Frag f1_ = ldfrag(base, 1);
    mma(f0_);
    ILV_MFMA_DS();
    SCHED();
    Frag f2_ = ldfrag(base, 2);
    mma(f1_);
    ILV_MFMA_DS();
    SCHED();
    Frag f3_ = ldfrag(base, 3);
    mma(f2_);
    ILV_MFMA_DS();
    SCHED();
    mma(f3_);
  }
  __builtin_amdgcn_s_barrier();
  constexpr int HROW = 144;
#pragma unroll
  for (int mi = 0; mi < 2; ++mi) {
    char* hp = lds + (wm * 64 + mi * 32 + r) * HROW + wn * 64 + 8 * hh;
#pragma unroll
    for (int gq = 0; gq < 4; ++gq) {
      float hv[4];
#pragma unroll
      for (int e = 0; e < 4; ++e) {
        const float gv = rbf(ag[mi][4 * gq + e]), uv = rbf(au[mi][4 * gq + e]);
        const float sg = rbf(__builtin_amdgcn_rcpf(1.0f + __builtin_amdgcn_exp2f(gv * -1.44269504f)));
        hv[e] = sg * uv;
      }
      u32x2 o = {pack2(hv[0], hv[1]), pack2(hv[2], hv[3])};
      *(u32x2*)(hp + 16 * gq) = o;
    }
  }
  WAIT_L(0);
  __builtin_amdgcn_s_barrier();
#pragma unroll
  for (int i = 0; i < 4; ++i) {
    const int row = w * 32 + i * 8 + (lane >> 3);
    u32x4 o = *(const u32x4*)(lds + row * HROW + (((lane & 7) ^ ((row >> 1) & 7)) * 16));
    *(u32x4*)(hout + ((size_t)nt * 256 + mt * 32 + w * 4 + i) * 512 + lane * 8) = o;
  }
}

constexpr int G2_A = 64 * 64 * 2, G2_B = 64 * 80 * 2, G2_ST = G2_A + G2_B, G2_NS = 6;

__global__ __launch_bounds__(512) void k_gemm2(const u16* __restrict__ hin, const u16* __restrict__ wpk,
                                               const u16* __restrict__ xres, u16* __restrict__ out) {
  __shared__ __attribute__((aligned(1024))) char lds[G2_NS * G2_ST];
  const int id = blockIdx.x;
  const int g = id & 7, j = id >> 3;
  const int mt = g * 4 + (j & 3), nt = j >> 2;
  const int m0 = mt * 64, n0 = nt * 80;
  const int tid = threadIdx.x, lane = tid & 63;
  const int w = __builtin_amdgcn_readfirstlane(tid >> 6);
  constexpr int NT = F / 64;
  if (w >= 4) {
    const int lw = w - 4;
    const unsigned lbase = lds_addr(lds);
    const unsigned voff = lane * 16;
    const char* abase = (const char*)hin + (size_t)(mt * 8 + lw * 2) * 1024;
    const char* bbase = (const char*)wpk + (size_t)nt * 27 * 10240 + lw * 1024;
    auto stage = [&](int buf, int kt) {
      const unsigned a = lbase + buf * G2_ST;
      const char* ab = abase + (size_t)kt * 256 * 1024;
      const char* bb = bbase + kt * 10240;
#pragma unroll
      for (int i = 0; i < 2; ++i) glds16(ab + i * 1024, voff, a + (lw * 2 + i) * 1024);
#pragma unroll
      for (int i = 0; i < 2; ++i) glds16(bb + i * 4096, voff, a + G2_A + (lw + 4 * i) * 1024);
      if (lw < 2) glds16(bb + 8192, voff, a + G2_A + (lw + 8) * 1024);
    };
#pragma unroll
    for (int i = 0; i < G2_NS; ++i) stage(i, i);
    if (lw < 2) WAIT_V(25); else WAIT_V(20);
    __builtin_amdgcn_s_barrier();
    int buf = 0;
#pragma unroll 1
    for (int kt = 0; kt + 1 < NT; ++kt) {
      const int ahead = (NT - 1 - (kt + 1)) < 4 ? (NT - 1 - (kt + 1)) : 4;
      if (lw < 2) {
        if (ahead == 4) WAIT_V(20); else if (ahead == 3) WAIT_V(15); else if (ahead == 2) WAIT_V(10); else if (ahead == 1) WAIT_V(5); else WAIT_V(0);
      } else {
        if (ahead == 4) WAIT_V(16); else if (ahead == 3) WAIT_V(12); else if (ahead == 2) WAIT_V(8); else if (ahead == 1) WAIT_V(4); else WAIT_V(0);
      }
      __builtin_amdgcn_s_barrier();
      if (kt + G2_NS < NT) stage(buf, kt + G2_NS);
      buf = (buf == G2_NS - 1) ? 0 : buf + 1;
    }
    return;
  }
  const int wm = w & 1, kh = w >> 1;
  f32x4 acc[2][5] = {};
  const int r = lane & 15, g4 = lane >> 4;
  const int aoff0 = (wm * 32 + r) * 128 + (((4 * kh + g4) ^ ((r >> 1) & 7)) * 16);
  const int q = (lane & 15) >> 2, p = lane & 3;
  const int boff = G2_A + (32 * kh + 8 * g4 + q) * 160 + p * 8;
  struct Frag { s16x8 a[2], b[5]; };
  auto ldfrag = [&](const char* base) -> Frag {
    Frag f;
    f.a[0] = *(const s16x8*)(base + aoff0);
    f.a[1] = *(const s16x8*)(base + aoff0 + 2048);
#pragma unroll
    for (int c = 0; c < 5; ++c) f.b[c] = tr_frag(base + boff + c * 32, base + boff + c * 32 + 640);
    return f;
  };
  auto mma = [&](const Frag& f) {
#pragma unroll
    for (int c = 0; c < 5; ++c) {
      acc[0][c] = __builtin_amdgcn_mfma_f32_16x16x32_bf16(f.b[c], f.a[0], acc[0][c], 0, 0, 0);
      acc[1][c] = __builtin_amdgcn_mfma_f32_16x16x32_bf16(f.b[c], f.a[1], acc[1][c], 0, 0, 0);
    }
  };
  __builtin_amdgcn_s_barrier();
  Frag fa = ldfrag(lds);
  int buf = 0;
#pragma unroll 1
  for (int kt = 0; kt < NT - 1; kt += 2) {
    buf = (buf == G2_NS - 1) ? 0 : buf + 1;
    WAIT_L(0);
    __builtin_amdgcn_s_barrier();
    SCHED();
    Frag fb = ldfrag(lds + buf * G2_ST);
    mma(fa);
#pragma unroll
    for (int i = 0; i < 6; ++i) { __builtin_amdgcn_sched_group_barrier(0x008, 1, 0); __builtin_amdgcn_sched_group_barrier(0x100, 2, 0); }
    __builtin_amdgcn_sched_group_barrier(0x008, 4, 0);
    SCHED();
    buf = (buf == G2_NS - 1) ? 0 : buf + 1;
    WAIT_L(0);
    __builtin_amdgcn_s_barrier();
    SCHED();
    fa = ldfrag(lds + buf * G2_ST);
    mma(fb);
#pragma unroll
    for (int i = 0; i < 6; ++i) { __builtin_amdgcn_sched_group_barrier(0x008, 1, 0); __builtin_amdgcn_sched_group_barrier(0x100, 2, 0); }
    __builtin_amdgcn_sched_group_barrier(0x008, 4, 0);
    SCHED();
  }
  mma(fa);
  __builtin_amdgcn_s_barrier();
  constexpr int PROW = 336;
  if (kh == 1) {
#pragma unroll
    for (int mi = 0; mi < 2; ++mi)
#pragma unroll
      for (int c = 0; c < 5; ++c)
        *(f32x4*)(lds + (wm * 32 + mi * 16 + r) * PROW + (c * 16 + 4 * g4) * 4) = acc[mi][c];
  }
  WAIT_L(0);
  __builtin_amdgcn_s_barrier();
  if (kh == 0) {
#pragma unroll
    for (int mi = 0; mi < 2; ++mi) {
      const int row = m0 + wm * 32 + mi * 16 + r;
#pragma unroll
      for (int c = 0; c < 5; ++c) {
        f32x4 o = *(const f32x4*)(lds + (wm * 32 + mi * 16 + r) * PROW + (c * 16 + 4 * g4) * 4);
        const size_t idx = (size_t)row * D + n0 + c * 16 + 4 * g4;
        u32x2 xr = *(const u32x2*)(xres + idx);
        u32x2 ov = {pack2(bflo(xr[0]) + rbf(acc[mi][c][0] + o[0]), bfhi(xr[0]) + rbf(acc[mi][c][1] + o[1])),
                    pack2(bflo(xr[1]) + rbf(acc[mi][c][2] + o[2]), bfhi(xr[1]) + rbf(acc[mi][c][3] + o[3]))};
        *(u32x2*)(out + idx) = ov;
      }
    }
  }
}

extern "C" void kernel_launch(void* const* d_in, const int* in_sizes, int n_in, void* d_out, int out_size,
                              void* d_ws, size_t ws_size, hipStream_t stream) {
  const u16* q = (const u16*)d_in[0];
  const u16* k = (const u16*)d_in[1];
  const u16* v = (const u16*)d_in[2];
  const u16* shift_k = (const u16*)d_in[3];
  const u16* ssm_k = (const u16*)d_in[4];
  const u16* ssm_D = (const u16*)d_in[5];
  const u16* wg = (const u16*)d_in[7];
  const u16* wu = (const u16*)d_in[8];
  const u16* wd = (const u16*)d_in[9];
  u16* out = (u16*)d_out;
  char* ws = (char*)d_ws;
  constexpr size_t SZ = (size_t)S * D * 2;
  u16* kvT = (u16*)(ws + 0 * SZ);
  u16* YT = (u16*)(ws + 1 * SZ);
  u16* Y = (u16*)(ws + 2 * SZ);
  u16* x1 = (u16*)(ws + 3 * SZ);
  u16* xn = (u16*)(ws + 4 * SZ);
  u16* x2 = (u16*)(ws + 5 * SZ);
  u16* x3 = (u16*)(ws + 6 * SZ);
  u16* h = (u16*)(ws + 7 * SZ);
  u16* wpk1 = (u16*)(ws + 10 * SZ);
  u16* wpk2 = (u16*)(ws + 12 * SZ);

  k_prep<<<dim3(S / 64, D / 64), 256, 0, stream>>>(k, v, shift_k, kvT);
  k_conv<<<D + PK_WGS, 256, 0, stream>>>(kvT, ssm_k, ssm_D, YT, wg, wu, wd, wpk1, wpk2);
  k_row<0><<<S / 8, 512, 0, stream>>>(q, nullptr, YT, Y, x1, xn);
  k_gemm1<<<256, 768, 0, stream>>>(xn, wpk1, h);
  k_gemm2<<<256, 512, 0, stream>>>(h, wpk2, x1, x2);
  k_row<1><<<S / 8, 512, 0, stream>>>(x2, nullptr, YT, Y, x3, xn);
  k_gemm1<<<256, 768, 0, stream>>>(xn, wpk1, h);
  k_gemm2<<<256, 512, 0, stream>>>(h, wpk2, x3, out);
}
```

```cpp
#include <hip/hip_runtime.h>
#include <hip/hip_bf16.h>
#include <type_traits>

typedef unsigned short u16;
typedef __attribute__((ext_vector_type(4))) short s16x4;
typedef __attribute__((ext_vector_type(8))) short s16x8;
typedef __attribute__((ext_vector_type(16))) float f32x16;
typedef __attribute__((ext_vector_type(4))) float f32x4;
typedef __attribute__((ext_vector_type(4))) unsigned u32x4;
typedef __attribute__((ext_vector_type(2))) unsigned u32x2;
typedef __attribute__((address_space(3))) s16x4* lds_s16x4_ptr;

constexpr int S = 2048, D = 640, F = 1728;

__device__ __forceinline__ float bf2f(u16 b) { return __uint_as_float((unsigned)b << 16); }
__device__ __forceinline__ float bflo(unsigned p) { return __uint_as_float(p << 16); }
__device__ __forceinline__ float bfhi(unsigned p) { return __uint_as_float(p & 0xffff0000u); }
__device__ __forceinline__ u16 f2bf(float x) { __bf16 b = (__bf16)x; return __builtin_bit_cast(u16, b); }
__device__ __forceinline__ float rbf(float x) { return bf2f(f2bf(x)); }
__device__ __forceinline__ unsigned pack2(float lo, float hi) { return (unsigned)f2bf(lo) | ((unsigned)f2bf(hi) << 16); }

typedef __attribute__((ext_vector_type(2))) float f32x2;
typedef __attribute__((ext_vector_type(2))) __bf16 bf16x2_t;
__device__ __forceinline__ unsigned cvtpk(f32x2 v) { return __builtin_bit_cast(unsigned, __builtin_convertvector(v, bf16x2_t)); }
__device__ __forceinline__ f32x2 unpk(unsigned p) { f32x2 r = {bflo(p), bfhi(p)}; return r; }
__device__ __forceinline__ f32x2 rbf2(f32x2 v) { return unpk(cvtpk(v)); }

#define WAIT_V(n) asm volatile("s_waitcnt vmcnt(%0)" ::"n"(n) : "memory")
#define WAIT_L(n) asm volatile("s_waitcnt lgkmcnt(%0)" ::"n"(n) : "memory")

constexpr int PK1_CHUNKS = 27 * 10 * 2 * 64 * 8, PK2_CHUNKS = 8 * 27 * 64 * 10;
constexpr int PK_WGS = (PK1_CHUNKS + PK2_CHUNKS) / 1024;
__device__ __forceinline__ void pack_weights(int wgi, const u16* __restrict__ wg, const u16* __restrict__ wu,
                                             const u16* __restrict__ wd, u16* __restrict__ wpk1, u16* __restrict__ wpk2) {
  u32x4 v[4];
#pragma unroll
  for (int i = 0; i < 4; ++i) {
    const int c = wgi * 1024 + i * 256 + threadIdx.x;
    if (c < PK1_CHUNKS) {
      const int cp = c & 7, k = (c >> 3) & 63, gu = (c >> 9) & 1, tk = c >> 10;
      const int nt = tk / 10, kt = tk - nt * 10;
      const int col = nt * 64 + (((cp >> 2) ^ ((k >> 1) & 1)) * 32) + (cp & 3) * 8;
      v[i] = *(const u32x4*)((gu ? wu : wg) + (size_t)(kt * 64 + k) * F + col);
    } else {
      const int c2 = c - PK1_CHUNKS;
      const int rk = c2 / 10, ch = c2 - rk * 10;
      const int tk = rk >> 6, k = rk & 63;
      const int nt = tk / 27, kt = tk - nt * 27;
      v[i] = *(const u32x4*)(wd + (size_t)(kt * 64 + k) * D + nt * 80 + ch * 8);
    }
  }
#pragma unroll
  for (int i = 0; i < 4; ++i) {
    const int c = wgi * 1024 + i * 256 + threadIdx.x;
    if (c < PK1_CHUNKS) *(u32x4*)(wpk1 + (size_t)c * 8) = v[i];
    else *(u32x4*)(wpk2 + (size_t)(c - PK1_CHUNKS) * 8) = v[i];
  }
}

__global__ __launch_bounds__(256) void k_prep(const u16* __restrict__ k, const u16* __restrict__ v,
                                              const u16* __restrict__ shift_k, u16* __restrict__ kvT,
                                              const u16* __restrict__ wg, const u16* __restrict__ wu,
                                              const u16* __restrict__ wd, u16* __restrict__ wpk1, u16* __restrict__ wpk2,
                                              unsigned* __restrict__ cnt) {
  __shared__ __attribute__((aligned(16))) unsigned tile[64][36];
  if (blockIdx.x == 0 && threadIdx.x < 64) cnt[threadIdx.x * 64] = 0u;
  if (blockIdx.x >= 320) { pack_weights(blockIdx.x - 320, wg, wu, wd, wpk1, wpk2); return; }
  const int t0 = (blockIdx.x & 31) * 64, d0 = (blockIdx.x >> 5) * 64;
  const int tid = threadIdx.x;
  const int cd = (tid & 7) * 8, tp = tid >> 3;
  const int t = t0 + 2 * tp;
  u32x4 kk[5], vv[2], sw[4];
#pragma unroll
  for (int j = 0; j < 5; ++j) {
    u32x4 z = {0u, 0u, 0u, 0u};
    kk[j] = (t - 3 + j >= 0) ? *(const u32x4*)(k + (size_t)(t - 3 + j) * D + d0 + cd) : z;
  }
  vv[0] = *(const u32x4*)(v + (size_t)t * D + d0 + cd);
  vv[1] = *(const u32x4*)(v + (size_t)(t + 1) * D + d0 + cd);
#pragma unroll
  for (int i = 0; i < 4; ++i) sw[i] = *(const u32x4*)(shift_k + (size_t)(d0 + cd) * 4 + 8 * i);
#pragma unroll
  for (int cp = 0; cp < 4; ++cp) {
    f32x2 wj[4];
    wj[0] = (f32x2){bflo(sw[cp][0]), bflo(sw[cp][2])};
    wj[1] = (f32x2){bfhi(sw[cp][0]), bfhi(sw[cp][2])};
    wj[2] = (f32x2){bflo(sw[cp][1]), bflo(sw[cp][3])};
    wj[3] = (f32x2){bfhi(sw[cp][1]), bfhi(sw[cp][3])};
    f32x2 kf[5];
#pragma unroll
    for (int j = 0; j < 5; ++j) kf[j] = unpk(kk[j][cp]);
    unsigned o[2];
#pragma unroll
    for (int rr = 0; rr < 2; ++rr) {
      f32x2 ks = rbf2(kf[rr + 0] * wj[3]);
      ks = rbf2(ks + rbf2(kf[rr + 1] * wj[2]));
      ks = rbf2(ks + rbf2(kf[rr + 2] * wj[1]));
      ks = rbf2(ks + rbf2(kf[rr + 3] * wj[0]));
      o[rr] = cvtpk(ks * unpk(vv[rr][cp]));
    }
    tile[cd + 2 * cp][tp] = (o[0] & 0xffffu) | (o[1] << 16);
    tile[cd + 2 * cp + 1][tp] = (o[0] >> 16) | (o[1] & 0xffff0000u);
  }
  __syncthreads();
  {
    const int dl = tid >> 2, c = (tid & 3) * 8;
    u32x4 a = *(const u32x4*)&tile[dl][c];
    u32x4 b = *(const u32x4*)&tile[dl][c + 4];
    u16* dst = kvT + (size_t)(d0 + dl) * S + t0 + 2 * c;
    *(u32x4*)dst = a;
    *(u32x4*)(dst + 8) = b;
  }
}

constexpr int CV_T_BYTES = 2112 * 8;
constexpr int CV_U_OFF = 32768;
constexpr int CV_U_ROW = 144;
constexpr int CV_LDS = CV_U_OFF + 64 * CV_U_ROW;

__global__ __launch_bounds__(256) void k_conv(const u16* __restrict__ kvT, const u16* __restrict__ ssm_k,
                                              const u16* __restrict__ ssm_D, u16* __restrict__ YT) {
  __shared__ __attribute__((aligned(16))) char lds[CV_LDS];
  const int d = blockIdx.x;
  const int tid = threadIdx.x, lane = tid & 63, w = tid >> 6;
  const u16* h = ssm_k + (size_t)d * S;
  const u16* u = kvT + (size_t)d * S;
  {
    const int base = 2040 - 8 * tid;
    u32x4 hi = *(const u32x4*)(h + base);
    u32x4 z = {0u, 0u, 0u, 0u};
    u32x4 lo = (base >= 8) ? *(const u32x4*)(h + base - 8) : z;
    unsigned Dw[8] = {lo[0], lo[1], lo[2], lo[3], hi[0], hi[1], hi[2], hi[3]};
    unsigned ent[16];
#pragma unroll
    for (int c = 0; c < 4; ++c) {
      ent[4 * c + 0] = __builtin_amdgcn_alignbit(Dw[7 - c], Dw[7 - c], 16);
      ent[4 * c + 1] = __builtin_amdgcn_alignbit(Dw[6 - c], Dw[6 - c], 16);
      ent[4 * c + 2] = (Dw[7 - c] & 0xffffu) | (Dw[6 - c] & 0xffff0000u);
      ent[4 * c + 3] = (Dw[6 - c] & 0xffffu) | (Dw[5 - c] & 0xffff0000u);
    }
    u32x4* tp = (u32x4*)(lds + tid * 64);
#pragma unroll
    for (int c = 0; c < 4; ++c) {
      u32x4 q = {ent[4 * c], ent[4 * c + 1], ent[4 * c + 2], ent[4 * c + 3]};
      tp[c] = q;
    }
    if (tid < 32) *(u32x4*)(lds + 2048 * 8 + tid * 16) = z;
    u32x4 uu = *(const u32x4*)(u + tid * 8);
    *(u32x4*)(lds + CV_U_OFF + (32 + (tid >> 3)) * CV_U_ROW + (tid & 7) * 16) = uu;
    for (int i = tid; i < 32 * CV_U_ROW / 16; i += 256) *(u32x4*)(lds + CV_U_OFF + i * 16) = z;
  }
  __syncthreads();
  const int r = lane & 31, hh = lane >> 5;
  f32x16 acc0 = {}, acc1 = {};
  {
    const char* tb = lds + (2047 - r + 8 * hh) * 8;
    const char* ub = lds + CV_U_OFF + (r + 32) * CV_U_ROW + hh * 16;
    auto ldT = [&](int q) -> s16x8 {
      const char* p = tb - q * 128;
      s16x4 a = *(const s16x4*)p;
      s16x4 b = *(const s16x4*)(p + 32);
      return __builtin_shufflevector(a, b, 0, 1, 2, 3, 4, 5, 6, 7);
    };
    const int m0 = 8 * w;
    s16x8 Tm3 = ldT(4 * m0 - 3), Tm2 = ldT(4 * m0 - 2);
#pragma unroll 2
    for (int mm = 0; mm < 8; ++mm) {
      const int m = m0 + mm;
      s16x8 Tm1 = ldT(4 * m - 1), T0 = ldT(4 * m), T1 = ldT(4 * m + 1), T2 = ldT(4 * m + 2);
      const char* up = ub - m * CV_U_ROW;
      s16x8 U0 = *(const s16x8*)(up), U1 = *(const s16x8*)(up + 32), U2 = *(const s16x8*)(up + 64), U3 = *(const s16x8*)(up + 96);
      acc0 = __builtin_amdgcn_mfma_f32_32x32x16_bf16(U0, T0, acc0, 0, 0, 0);
      acc1 = __builtin_amdgcn_mfma_f32_32x32x16_bf16(U0, T2, acc1, 0, 0, 0);
      acc0 = __builtin_amdgcn_mfma_f32_32x32x16_bf16(U1, Tm1, acc0, 0, 0, 0);
      acc1 = __builtin_amdgcn_mfma_f32_32x32x16_bf16(U1, T1, acc1, 0, 0, 0);
      acc0 = __builtin_amdgcn_mfma_f32_32x32x16_bf16(U2, Tm2, acc0, 0, 0, 0);
      acc1 = __builtin_amdgcn_mfma_f32_32x32x16_bf16(U2, T0, acc1, 0, 0, 0);
      acc0 = __builtin_amdgcn_mfma_f32_32x32x16_bf16(U3, Tm3, acc0, 0, 0, 0);
      acc1 = __builtin_amdgcn_mfma_f32_32x32x16_bf16(U3, Tm1, acc1, 0, 0, 0);
      Tm3 = T1; Tm2 = T2;
    }
  }
  asm volatile("" :: "v"(acc0[0]), "v"(acc1[0]));
  __syncthreads();
  {
    float* P = (float*)lds + w * 2048;
#pragma unroll
    for (int reg = 0; reg < 16; ++reg) {
      const int i = (reg & 3) + 8 * (reg >> 2) + 4 * hh;
      P[64 * i + r] = acc0[reg];
      P[64 * i + 32 + r] = acc1[reg];
    }
  }
  __syncthreads();
  {
    const float* P = (const float*)lds + tid * 8;
    float s[8];
#pragma unroll
    for (int e = 0; e < 8; ++e) s[e] = 0.f;
#pragma unroll
    for (int ww = 0; ww < 4; ++ww) {
      f32x4 a = *(const f32x4*)(P + ww * 2048), b = *(const f32x4*)(P + ww * 2048 + 4);
#pragma unroll
      for (int e = 0; e < 4; ++e) { s[e] += a[e]; s[4 + e] += b[e]; }
    }
    u32x4 uu = *(const u32x4*)(lds + CV_U_OFF + (32 + (tid >> 3)) * CV_U_ROW + (tid & 7) * 16);
    const float Dv = bf2f(ssm_D[d]);
    u32x4 o;
#pragma unroll
    for (int e = 0; e < 4; ++e) {
      float y0 = rbf(s[2 * e]) + rbf(bflo(uu[e]) * Dv);
      float y1 = rbf(s[2 * e + 1]) + rbf(bfhi(uu[e]) * Dv);
      o[e] = pack2(y0, y1);
    }
    *(u32x4*)(YT + (size_t)d * S + tid * 8) = o;
  }
}

template <int CTRL> __device__ __forceinline__ float dpp_add(float v) {
  return v + __builtin_bit_cast(float, __builtin_amdgcn_update_dpp(0, __builtin_bit_cast(int, v), CTRL, 0xf, 0xf, true));
}
__device__ __forceinline__ float wave_sum(float v) {
  v = dpp_add<0xB1>(v);
  v = dpp_add<0x4E>(v);
  v = dpp_add<0x141>(v);
  v = dpp_add<0x140>(v);
  const int iv = __builtin_bit_cast(int, v);
  const float a = __builtin_bit_cast(float, __builtin_amdgcn_readlane(iv, 0));
  const float b = __builtin_bit_cast(float, __builtin_amdgcn_readlane(iv, 16));
  const float c = __builtin_bit_cast(float, __builtin_amdgcn_readlane(iv, 32));
  const float d = __builtin_bit_cast(float, __builtin_amdgcn_readlane(iv, 48));
  return (a + b) + (c + d);
}
__device__ __forceinline__ void ln_row(const f32x2 (&x)[5], unsigned (&o)[5]) {
  f32x2 s2 = (x[0] + x[1]) + (x[2] + x[3]) + x[4];
  const float mean = wave_sum(s2[0] + s2[1]) * (1.0f / D);
  f32x2 q2 = {0.f, 0.f};
#pragma unroll
  for (int i = 0; i < 5; ++i) { f32x2 dd = x[i] - mean; q2 += dd * dd; }
  const float var = rbf(wave_sum(q2[0] + q2[1]) * (1.0f / D));
  const float mb = rbf(mean);
  const float sd = rbf(__builtin_amdgcn_sqrtf(rbf(var + 1e-5f)));
  const float rs = __builtin_amdgcn_rcpf(sd);
#pragma unroll
  for (int i = 0; i < 5; ++i) o[i] = cvtpk(rbf2(x[i] - mb) * rs);
}

__device__ __forceinline__ unsigned lds_addr(const void* p) {
  return (unsigned)(uintptr_t)(const __attribute__((address_space(3))) char*)p;
}
template <bool SC1 = false>
__device__ __forceinline__ void glds16(const void* sbase, unsigned voff, unsigned ldst) {
  const unsigned long long pv = (unsigned long long)sbase;
  const unsigned long long ub = ((unsigned long long)(unsigned)__builtin_amdgcn_readfirstlane((unsigned)(pv >> 32)) << 32) |
                                (unsigned)__builtin_amdgcn_readfirstlane((unsigned)pv);
  const unsigned ul = __builtin_amdgcn_readfirstlane(ldst);
  unsigned keep;
  if (SC1)
    asm volatile("s_mov_b32 %0, m0\n\ts_mov_b32 m0, %3\n\ts_nop 0\n\tglobal_load_lds_dwordx4 %1, %2 sc1\n\ts_mov_b32 m0, %0"
                 : "=&s"(keep) : "v"(voff), "s"(ub), "s"(ul) : "memory");
  else
    asm volatile("s_mov_b32 %0, m0\n\ts_mov_b32 m0, %3\n\ts_nop 0\n\tglobal_load_lds_dwordx4 %1, %2\n\ts_mov_b32 m0, %0"
                 : "=&s"(keep) : "v"(voff), "s"(ub), "s"(ul) : "memory");
}
__device__ __forceinline__ s16x8 tr_frag(const char* p0, const char* p1) {
  s16x4 a = __builtin_amdgcn_ds_read_tr16_b64_v4i16((lds_s16x4_ptr)p0);
  s16x4 b = __builtin_amdgcn_ds_read_tr16_b64_v4i16((lds_s16x4_ptr)p1);
  return __builtin_shufflevector(a, b, 0, 1, 2, 3, 4, 5, 6, 7);
}

#define RLX_AGENT __ATOMIC_RELAXED, __HIP_MEMORY_SCOPE_AGENT
__device__ __forceinline__ void st16_sc1(void* p, u32x4 v) { asm volatile("global_store_dwordx4 %0, %1, off sc1\n\ts_nop 1" ::"v"(p), "v"(v) : "memory"); }
__device__ __forceinline__ void st8_sc1(void* p, u32x2 v) { asm volatile("global_store_dwordx2 %0, %1, off sc1\n\ts_nop 1" ::"v"(p), "v"(v) : "memory"); }
__device__ __forceinline__ void wg_signal(unsigned* cnt) {
  asm volatile("s_waitcnt vmcnt(0) lgkmcnt(0)" ::: "memory");
  __builtin_amdgcn_s_barrier();
  if (threadIdx.x == 0) __hip_atomic_fetch_add(cnt, 1u, RLX_AGENT);
}
__device__ __forceinline__ void wg_wait(unsigned* cnt, unsigned want) {
  if (threadIdx.x < 64) {
    unsigned spins = 0;
    while ((unsigned)__builtin_amdgcn_readfirstlane(__hip_atomic_load(cnt, RLX_AGENT)) < want) {
      __builtin_amdgcn_s_sleep(1);
      if (++spins > (1u << 22)) break;
    }
  }
  asm volatile("" ::: "memory");
  __builtin_amdgcn_s_barrier();
  asm volatile("" ::: "memory");
}

#define SCHED() __builtin_amdgcn_sched_barrier(0)
#define ILV_MFMA_DS()                                        \
  do {                                                       \
    __builtin_amdgcn_sched_group_barrier(0x008, 1, 0);       \
    __builtin_amdgcn_sched_group_barrier(0x100, 2, 0);       \
    __builtin_amdgcn_sched_group_barrier(0x008, 1, 0);       \
    __builtin_amdgcn_sched_group_barrier(0x100, 2, 0);       \
    __builtin_amdgcn_sched_group_barrier(0x008, 1, 0);       \
    __builtin_amdgcn_sched_group_barrier(0x100, 2, 0);       \
    __builtin_amdgcn_sched_group_barrier(0x008, 1, 0);       \
  } while (0)

__device__ __forceinline__ unsigned quad_get(unsigned v, int sel) {
  switch (sel) {
    case 1: return __builtin_amdgcn_update_dpp(0, (int)v, 0x55, 0xf, 0xf, true);
    case 2: return __builtin_amdgcn_update_dpp(0, (int)v, 0xAA, 0xf, 0xf, true);
    default: return __builtin_amdgcn_update_dpp(0, (int)v, 0xFF, 0xf, 0xf, true);
  }
}
template <int MODE>
__device__ __forceinline__ void row_phase(char* lds, int t0, int w, int lane, const u16* __restrict__ xin,
                                          const u16* __restrict__ YT, u16* __restrict__ Y,
                                          u16* __restrict__ xout, u16* __restrict__ xn) {
  u16 (*yt)[D + 8] = (u16 (*)[D + 8])lds;
  const int t = t0 + (w & 7);
  u32x4 xa = {0u, 0u, 0u, 0u}; unsigned xb = 0u;
  if (w < 8) {
    if (MODE == 0) {
      xa = *(const u32x4*)(xin + (size_t)t * D + 8 * lane);
      xb = *(const unsigned*)(xin + (size_t)t * D + 512 + 2 * lane);
    } else {
      const unsigned long long* xp = (const unsigned long long*)(xin + (size_t)t * D + 8 * lane);
      const unsigned long long v0 = __hip_atomic_load(xp, RLX_AGENT), v1 = __hip_atomic_load(xp + 1, RLX_AGENT);
      xa = (u32x4){(unsigned)v0, (unsigned)(v0 >> 32), (unsigned)v1, (unsigned)(v1 >> 32)};
      xb = __hip_atomic_load((const unsigned*)(xin + (size_t)t * D + 512 + 2 * lane), RLX_AGENT);
    }
  }
  u32x4 ya; unsigned yb;
  if (MODE == 0) {
    const int dd = w * 64 + lane;
    if (dd < D) {
      u32x4 q = *(const u32x4*)(YT + (size_t)dd * S + t0);
#pragma unroll
      for (int e = 0; e < 4; ++e) {
        yt[2 * e][dd] = (u16)(q[e] & 0xffffu);
        yt[2 * e + 1][dd] = (u16)(q[e] >> 16);
      }
    }
    __syncthreads();
  }
  if (w < 8) {
    if (MODE == 0) {
      ya = *(const u32x4*)&yt[w][8 * lane];
      yb = *(const unsigned*)&yt[w][512 + 2 * lane];
      *(u32x4*)(Y + (size_t)t * D + 8 * lane) = ya;
      *(unsigned*)(Y + (size_t)t * D + 512 + 2 * lane) = yb;
    } else {
      ya = *(const u32x4*)(Y + (size_t)t * D + 8 * lane);
      yb = *(const unsigned*)(Y + (size_t)t * D + 512 + 2 * lane);
    }
    f32x2 x[5] = {unpk(xa[0]), unpk(xa[1]), unpk(xa[2]), unpk(xa[3]), unpk(xb)};
    const unsigned yp[5] = {ya[0], ya[1], ya[2], ya[3], yb};
    unsigned n[5], xo[5];
    ln_row(x, n);
#pragma unroll
    for (int i = 0; i < 5; ++i) { xo[i] = cvtpk(x[i] + rbf2(unpk(n[i]) * unpk(yp[i]))); x[i] = unpk(xo[i]); }
    ln_row(x, n);
    const u32x4 xt = {xo[4], quad_get(xo[4], 1), quad_get(xo[4], 2), quad_get(xo[4], 3)};
    const u32x4 nt4 = {n[4], quad_get(n[4], 1), quad_get(n[4], 2), quad_get(n[4], 3)};
    const int sw = (t >> 1) & 7;
    u16* bp = xn + (size_t)(t >> 8) * (10 * 256 * 64) + (t & 255) * 64;
    st16_sc1(xout + (size_t)t * D + 8 * lane, (u32x4){xo[0], xo[1], xo[2], xo[3]});
    st16_sc1(bp + (lane >> 3) * (256 * 64) + (((lane & 7) ^ sw) * 8), (u32x4){n[0], n[1], n[2], n[3]});
    if ((lane & 3) == 0) {
      st16_sc1(xout + (size_t)t * D + 512 + 2 * lane, xt);
      st16_sc1(bp + (8 + (lane >> 5)) * (256 * 64) + ((((lane & 31) >> 2) ^ sw) * 8), nt4);
    }
  }
}

constexpr int G1_A = 256 * 64 * 2, G1_B = 64 * 128 * 2, G1_ST = G1_A + G1_B, G1_NS = 3;

__device__ __forceinline__ void gemm1_phase(char* lds, int mt, int nt, int w, int lane, const u16* __restrict__ xn,
                                            const u16* __restrict__ wpk, u16* __restrict__ hout) {
  constexpr int NT = D / 64;
  if (w >= 8) {
    const int lw = w - 8;
    const unsigned lbase = lds_addr(lds);
    const unsigned voff = lane * 16;
    const char* abase = (const char*)xn + (size_t)mt * 10 * 32768 + lw * 8192;
    const char* bbase = (const char*)wpk + (size_t)nt * 10 * 16384 + lw * 4096;
    auto stage = [&](int buf, int kt) {
      const unsigned a = lbase + buf * G1_ST;
      const char* ab = abase + kt * 32768;
      const char* bb = bbase + kt * 16384;
#pragma unroll
      for (int i = 0; i < 4; ++i) glds16(bb + i * 1024, voff, a + G1_A + (lw * 4 + i) * 1024);
#pragma unroll
      for (int i = 0; i < 8; ++i) glds16<true>(ab + i * 1024, voff, a + (lw * 8 + i) * 1024);
    };
    stage(0, 0);
    stage(1, 1);
    WAIT_V(12);
    __builtin_amdgcn_s_barrier();
    stage(2, 2);
    int buf = 0;
#pragma unroll 1
    for (int kt = 0; kt + 3 < NT; ++kt) {
      WAIT_V(12);
      __builtin_amdgcn_s_barrier();
      stage(buf, kt + 3);
      buf = (buf == 2) ? 0 : buf + 1;
    }
    WAIT_V(12);
    __builtin_amdgcn_s_barrier();
    WAIT_V(0);
    __builtin_amdgcn_s_barrier();
    __builtin_amdgcn_s_barrier();
    __builtin_amdgcn_s_barrier();
  } else {
    const int wm = w >> 1, wn = w & 1;
    f32x16 ag[2] = {}, au[2] = {};
    const int r = lane & 31, hh = lane >> 5;
    const int arow = (wm * 64 + r) * 128;
    const int asw = (r >> 1) & 7;
    const int blk = (lane >> 4) & 1, q = (lane & 15) >> 2, p = lane & 3;
    const int boff = G1_A + (8 * hh + q) * 128 + ((wn ^ ((q >> 1) & 1)) * 64) + blk * 32 + p * 8;
    struct Frag { s16x8 a0, a1, bg, bu; };
    auto ldfrag = [&](const char* base, int ks) -> Frag {
      Frag f;
      const char* ap = base + arow + (((2 * ks + hh) ^ asw) * 16);
      const char* bp = base + boff + ks * 2048;
      f.bg = tr_frag(bp, bp + 512);
      f.a0 = *(const s16x8*)ap;
      f.bu = tr_frag(bp + 8192, bp + 8192 + 512);
      f.a1 = *(const s16x8*)(ap + 4096);
      return f;
    };
    auto mma = [&](const Frag& f) {
      ag[0] = __builtin_amdgcn_mfma_f32_32x32x16_bf16(f.bg, f.a0, ag[0], 0, 0, 0);
      au[0] = __builtin_amdgcn_mfma_f32_32x32x16_bf16(f.bu, f.a0, au[0], 0, 0, 0);
      ag[1] = __builtin_amdgcn_mfma_f32_32x32x16_bf16(f.bg, f.a1, ag[1], 0, 0, 0);
      au[1] = __builtin_amdgcn_mfma_f32_32x32x16_bf16(f.bu, f.a1, au[1], 0, 0, 0);
    };
    asm volatile("" ::: "memory");
    __builtin_amdgcn_s_barrier();
    asm volatile("" ::: "memory");
    Frag f0_ = ldfrag(lds, 0);
    int buf = 0;
#pragma unroll 1
    for (int kt = 0; kt < NT - 1; ++kt) {
      const char* base = lds + buf * G1_ST;
      SCHED();
      Frag f1_ = ldfrag(base, 1);
      mma(f0_);
      ILV_MFMA_DS();
      SCHED();
      Frag f2_ = ldfrag(base, 2);
      mma(f1_);
      ILV_MFMA_DS();
      SCHED();
      Frag f3_ = ldfrag(base, 3);
      mma(f2_);
      ILV_MFMA_DS();
      SCHED();
      buf = (buf == 2) ? 0 : buf + 1;
      WAIT_L(0);
      __builtin_amdgcn_s_barrier();
      asm volatile("" ::: "memory");
      SCHED();
      f0_ = ldfrag(lds + buf * G1_ST, 0);
      mma(f3_);
      ILV_MFMA_DS();
      SCHED();
    }
    {
      const char* base = lds + buf * G1_ST;
      Frag f1_ = ldfrag(base, 1);
      mma(f0_);
      ILV_MFMA_DS();
      SCHED();
      Frag f2_ = ldfrag(base, 2);
      mma(f1_);
      ILV_MFMA_DS();
      SCHED();
      Frag f3_ = ldfrag(base, 3);
      mma(f2_);
      ILV_MFMA_DS();
      SCHED();
      mma(f3_);
    }
    WAIT_L(0);
    __builtin_amdgcn_s_barrier();
    asm volatile("" ::: "memory");
    constexpr int HROW = 144;
#pragma unroll
    for (int mi = 0; mi < 2; ++mi) {
      char* hp = lds + (wm * 64 + mi * 32 + r) * HROW + wn * 64 + 8 * hh;
#pragma unroll
      for (int gq = 0; gq < 4; ++gq) {
        u32x2 o;
#pragma unroll
        for (int e = 0; e < 2; ++e) {
          const f32x2 gv = rbf2((f32x2){ag[mi][4 * gq + 2 * e], ag[mi][4 * gq + 2 * e + 1]}) * -1.44269504f;
          const f32x2 uv = rbf2((f32x2){au[mi][4 * gq + 2 * e], au[mi][4 * gq + 2 * e + 1]});
          const f32x2 den = (f32x2){__builtin_amdgcn_exp2f(gv[0]), __builtin_amdgcn_exp2f(gv[1])} + 1.0f;
          const f32x2 sg = rbf2((f32x2){__builtin_amdgcn_rcpf(den[0]), __builtin_amdgcn_rcpf(den[1])});
          o[e] = cvtpk(sg * uv);
        }
        *(u32x2*)(hp + 16 * gq) = o;
      }
    }
    WAIT_L(0);
    __builtin_amdgcn_s_barrier();
    asm volatile("" ::: "memory");
#pragma unroll
    for (int i = 0; i < 4; ++i) {
      const int row = w * 32 + i * 8 + (lane >> 3);
      u32x4 o = *(const u32x4*)(lds + row * HROW + (((lane & 7) ^ ((row >> 1) & 7)) * 16));
      st16_sc1(hout + ((size_t)(mt * 4 + (w >> 1)) * 27 + nt) * 4096 + ((w & 1) * 4 + i) * 512 + lane * 8, o);
    }
  }
}

constexpr int G2_A = 64 * 64 * 2, G2_B = 64 * 80 * 2, G2_ST = G2_A + G2_B, G2_NS = 6, G2_LAG = 1;

template <bool HANDOFF>
__device__ __forceinline__ void gemm2_phase(char* lds, int mt, int nt, int w, int lane, const u16* __restrict__ hin,
                                            const u16* __restrict__ wpk, const u16* __restrict__ xres,
                                            u16* __restrict__ out) {
  constexpr int NT = F / 64;
  const int m0 = mt * 64, n0 = nt * 80;
  if (w >= 4) {
    const int lw = w - 4;
    const unsigned lbase = lds_addr(lds);
    const unsigned voff = lane * 16;
    const char* abase = (const char*)hin + (size_t)mt * 27 * 8192 + lw * 1024;
    const char* bbase = (const char*)wpk + (size_t)nt * 27 * 10240 + lw * 1024;
    auto stage = [&](int buf, int kt) {
      const unsigned a = lbase + buf * G2_ST;
      glds16<true>(abase + kt * 8192, voff, a + lw * 1024);
      glds16(bbase + kt * 10240, voff, a + G2_A + lw * 1024);
      if (lw < 2) glds16(bbase + kt * 10240 + 8192, voff, a + G2_A + (lw + 8) * 1024);
    };
    stage(0, 0);
    stage(1, 1);
    if (lw < 2) WAIT_V(3); else WAIT_V(2);
    __builtin_amdgcn_s_barrier();
#pragma unroll
    for (int i = 2; i < G2_NS; ++i) stage(i, i);
    auto loader_loop = [&](auto per_tile_c) {
      constexpr int PT = decltype(per_tile_c)::value;
#pragma unroll
      for (int kt = 0; kt < G2_LAG; ++kt) { WAIT_V(PT * (G2_NS - 2 - kt)); __builtin_amdgcn_s_barrier(); }
      int buf = 0;
#pragma unroll 1
      for (int kt = G2_LAG; kt - G2_LAG + G2_NS < NT; ++kt) {
        WAIT_V(PT * (G2_NS - 2 - G2_LAG));
        __builtin_amdgcn_s_barrier();
        stage(buf, kt - G2_LAG + G2_NS);
        buf = (buf == G2_NS - 1) ? 0 : buf + 1;
      }
#pragma unroll
      for (int i = G2_NS - 1 - G2_LAG; i >= 1; --i) { WAIT_V(PT * (i - 1)); __builtin_amdgcn_s_barrier(); }
    };
    if (lw < 2) loader_loop(std::integral_constant<int, 3>{}); else loader_loop(std::integral_constant<int, 2>{});
    __builtin_amdgcn_s_barrier();
    __builtin_amdgcn_s_barrier();
  } else {
    const int wm = w & 1, kh = w >> 1;
    f32x4 acc[2][5] = {};
    const int r = lane & 15, g4 = lane >> 4;
    const int aoff0 = (wm * 32 + r) * 128 + (((4 * kh + g4) ^ ((r >> 1) & 7)) * 16);
    const int q = (lane & 15) >> 2, p = lane & 3;
    const int boff = G2_A + (32 * kh + 8 * g4 + q) * 160 + p * 8;
    struct Frag { s16x8 a[2], b[5]; };
    auto ldfrag = [&](const char* base) -> Frag {
      Frag f;
      f.a[0] = *(const s16x8*)(base + aoff0);
      f.a[1] = *(const s16x8*)(base + aoff0 + 2048);
#pragma unroll
      for (int c = 0; c < 5; ++c) f.b[c] = tr_frag(base + boff + c * 32, base + boff + c * 32 + 640);
      return f;
    };
    auto mma = [&](const Frag& f) {
#pragma unroll
      for (int c = 0; c < 5; ++c) {
        acc[0][c] = __builtin_amdgcn_mfma_f32_16x16x32_bf16(f.b[c], f.a[0], acc[0][c], 0, 0, 0);
        acc[1][c] = __builtin_amdgcn_mfma_f32_16x16x32_bf16(f.b[c], f.a[1], acc[1][c], 0, 0, 0);
      }
    };
    u32x2 xr[2][5];
    if (kh == 0) {
#pragma unroll
      for (int mi = 0; mi < 2; ++mi)
#pragma unroll
        for (int c = 0; c < 5; ++c) {
          const unsigned long long v = __hip_atomic_load((const unsigned long long*)(xres + (size_t)(m0 + wm * 32 + mi * 16 + r) * D + n0 + c * 16 + 4 * g4), RLX_AGENT);
          xr[mi][c] = (u32x2){(unsigned)v, (unsigned)(v >> 32)};
        }
    }
    asm volatile("" ::: "memory");
    __builtin_amdgcn_s_barrier();
    asm volatile("" ::: "memory");
    Frag fa = ldfrag(lds);
    int buf = 0;
#pragma unroll 1
    for (int kt = 0; kt < NT - 1; kt += 2) {
      buf = (buf == G2_NS - 1) ? 0 : buf + 1;
      __builtin_amdgcn_s_barrier();
      asm volatile("" ::: "memory");
      SCHED();
      Frag fb = ldfrag(lds + buf * G2_ST);
      mma(fa);
#pragma unroll
      for (int i = 0; i < 6; ++i) { __builtin_amdgcn_sched_group_barrier(0x008, 1, 0); __builtin_amdgcn_sched_group_barrier(0x100, 2, 0); }
      __builtin_amdgcn_sched_group_barrier(0x008, 4, 0);
      SCHED();
      buf = (buf == G2_NS - 1) ? 0 : buf + 1;
      __builtin_amdgcn_s_barrier();
      asm volatile("" ::: "memory");
      SCHED();
      fa = ldfrag(lds + buf * G2_ST);
      mma(fb);
#pragma unroll
      for (int i = 0; i < 6; ++i) { __builtin_amdgcn_sched_group_barrier(0x008, 1, 0); __builtin_amdgcn_sched_group_barrier(0x100, 2, 0); }
      __builtin_amdgcn_sched_group_barrier(0x008, 4, 0);
      SCHED();
    }
    mma(fa);
    WAIT_L(0);
    __builtin_amdgcn_s_barrier();
    asm volatile("" ::: "memory");
    constexpr int PROW = 336;
    if (kh == 1) {
#pragma unroll
      for (int mi = 0; mi < 2; ++mi)
#pragma unroll
        for (int c = 0; c < 5; ++c)
          *(f32x4*)(lds + (wm * 32 + mi * 16 + r) * PROW + (c * 16 + 4 * g4) * 4) = acc[mi][c];
    }
    WAIT_L(0);
    __builtin_amdgcn_s_barrier();
    asm volatile("" ::: "memory");
    if (kh == 0) {
#pragma unroll
      for (int mi = 0; mi < 2; ++mi) {
        const int row = m0 + wm * 32 + mi * 16 + r;
#pragma unroll
        for (int c = 0; c < 5; ++c) {
          f32x4 o = *(const f32x4*)(lds + (wm * 32 + mi * 16 + r) * PROW + (c * 16 + 4 * g4) * 4);
          const size_t idx = (size_t)row * D + n0 + c * 16 + 4 * g4;
          const u32x2 xv = xr[mi][c];
          u32x2 ov = {cvtpk(unpk(xv[0]) + rbf2((f32x2){acc[mi][c][0] + o[0], acc[mi][c][1] + o[1]})),
                      cvtpk(unpk(xv[1]) + rbf2((f32x2){acc[mi][c][2] + o[2], acc[mi][c][3] + o[3]}))};
          if (HANDOFF) st8_sc1(out + idx, ov); else *(u32x2*)(out + idx) = ov;
        }
      }
    }
  }
}

constexpr int CNT_STRIDE = 64;
constexpr int CNT_A = 0, CNT_B = 8, CNT_C = 16, CNT_D = 48, CNT_E = 56, CNT_N = 64;

struct MainArgs {
  const u16* q; const u16* YT; u16* Y; u16* x1; u16* x2; u16* x3; u16* xn1; u16* xn2; u16* h1; u16* h2;
  const u16* wpk1; const u16* wpk2; u16* out; unsigned* cnt;
};

__global__ __launch_bounds__(768) void k_main(MainArgs a) {
  __shared__ __attribute__((aligned(1024))) char lds[G1_NS * G1_ST];
  const int id = blockIdx.x;
  const int g = id & 7, j = id >> 3;
  const int tid = threadIdx.x, lane = tid & 63;
  const int w = __builtin_amdgcn_readfirstlane(tid >> 6);
  const int p64 = g * 4 + (j & 3), n2 = j >> 2;
  const int t0 = p64 * 64 + n2 * 8;
  const int pm = p64 >> 2;
  const int mt1 = (g & 1) * 4 + (j & 3), nj = j >> 2, nt1 = (g >> 1) * 7 + nj;
  const bool has1 = (nj < 7) && (nt1 < 27);
  unsigned* cnt = a.cnt;

  row_phase<0>(lds, t0, w, lane, a.q, a.YT, a.Y, a.x1, a.xn1);
  wg_signal(cnt + (CNT_A + pm) * CNT_STRIDE);
  if (has1) {
    wg_wait(cnt + (CNT_A + mt1) * CNT_STRIDE, 32);
    gemm1_phase(lds, mt1, nt1, w, lane, a.xn1, a.wpk1, a.h1);
    wg_signal(cnt + (CNT_B + mt1) * CNT_STRIDE);
  }
  wg_wait(cnt + (CNT_B + pm) * CNT_STRIDE, 27);
  gemm2_phase<true>(lds, p64, n2, w, lane, a.h1, a.wpk2, a.x1, a.x2);
  wg_signal(cnt + (CNT_C + p64) * CNT_STRIDE);
  wg_wait(cnt + (CNT_C + p64) * CNT_STRIDE, 8);
  row_phase<1>(lds, t0, w, lane, a.x2, a.YT, a.Y, a.x3, a.xn2);
  wg_signal(cnt + (CNT_D + pm) * CNT_STRIDE);
  if (has1) {
    wg_wait(cnt + (CNT_D + mt1) * CNT_STRIDE, 32);
    gemm1_phase(lds, mt1, nt1, w, lane, a.xn2, a.wpk1, a.h2);
    wg_signal(cnt + (CNT_E + mt1) * CNT_STRIDE);
  }
  wg_wait(cnt + (CNT_E + pm) * CNT_STRIDE, 27);
  gemm2_phase<false>(lds, p64, n2, w, lane, a.h2, a.wpk2, a.x3, a.out);
}

extern "C" void kernel_launch(void* const* d_in, const int* in_sizes, int n_in, void* d_out, int out_size,
                              void* d_ws, size_t ws_size, hipStream_t stream) {
  const u16* q = (const u16*)d_in[0];
  const u16* k = (const u16*)d_in[1];
  const u16* v = (const u16*)d_in[2];
  const u16* shift_k = (const u16*)d_in[3];
  const u16* ssm_k = (const u16*)d_in[4];
  const u16* ssm_D = (const u16*)d_in[5];
  const u16* wg = (const u16*)d_in[7];
  const u16* wu = (const u16*)d_in[8];
  const u16* wd = (const u16*)d_in[9];
  char* ws = (char*)d_ws;
  constexpr size_t SZ = (size_t)S * D * 2;
  u16* kvT = (u16*)(ws + 0 * SZ);
  u16* YT = (u16*)(ws + 1 * SZ);
  u16* wpk1 = (u16*)(ws + 10 * SZ);
  u16* wpk2 = (u16*)(ws + 12 * SZ);
  MainArgs a;
  a.q = q; a.YT = YT; a.Y = (u16*)(ws + 2 * SZ);
  a.x1 = (u16*)(ws + 3 * SZ); a.xn1 = (u16*)(ws + 4 * SZ); a.x2 = (u16*)(ws + 5 * SZ); a.x3 = (u16*)(ws + 6 * SZ);
  a.h1 = (u16*)(ws + 7 * SZ); a.xn2 = (u16*)(ws + 13 * SZ); a.h2 = (u16*)(ws + 14 * SZ);
  a.wpk1 = wpk1; a.wpk2 = wpk2; a.out = (u16*)d_out; a.cnt = (unsigned*)(ws + 17 * SZ);

  k_prep<<<320 + PK_WGS, 256, 0, stream>>>(k, v, shift_k, kvT, wg, wu, wd, wpk1, wpk2, a.cnt);
  k_conv<<<D, 256, 0, stream>>>(kvT, ssm_k, ssm_D, YT);
  k_main<<<256, 768, 0, stream>>>(a);
}
```

```cpp
#include <hip/hip_runtime.h>
#include <hip/hip_bf16.h>
#include <type_traits>

typedef unsigned short u16;
typedef __attribute__((ext_vector_type(4))) short s16x4;
typedef __attribute__((ext_vector_type(8))) short s16x8;
typedef __attribute__((ext_vector_type(16))) float f32x16;
typedef __attribute__((ext_vector_type(4))) float f32x4;
typedef __attribute__((ext_vector_type(4))) unsigned u32x4;
typedef __attribute__((ext_vector_type(2))) unsigned u32x2;
typedef __attribute__((address_space(3))) s16x4* lds_s16x4_ptr;

constexpr int S = 2048, D = 640, F = 1728;

__device__ __forceinline__ float bf2f(u16 b) { return __uint_as_float((unsigned)b << 16); }
__device__ __forceinline__ float bflo(unsigned p) { return __uint_as_float(p << 16); }
__device__ __forceinline__ float bfhi(unsigned p) { return __uint_as_float(p & 0xffff0000u); }
__device__ __forceinline__ u16 f2bf(float x) { __bf16 b = (__bf16)x; return __builtin_bit_cast(u16, b); }
__device__ __forceinline__ float rbf(float x) { return bf2f(f2bf(x)); }
__device__ __forceinline__ unsigned pack2(float lo, float hi) { return (unsigned)f2bf(lo) | ((unsigned)f2bf(hi) << 16); }

typedef __attribute__((ext_vector_type(2))) float f32x2;
typedef __attribute__((ext_vector_type(2))) __bf16 bf16x2_t;
__device__ __forceinline__ unsigned cvtpk(f32x2 v) { return __builtin_bit_cast(unsigned, __builtin_convertvector(v, bf16x2_t)); }
__device__ __forceinline__ f32x2 unpk(unsigned p) { f32x2 r = {bflo(p), bfhi(p)}; return r; }
__device__ __forceinline__ f32x2 rbf2(f32x2 v) { return unpk(cvtpk(v)); }

#define WAIT_V(n) asm volatile("s_waitcnt vmcnt(%0)" ::"n"(n) : "memory")
#define WAIT_L(n) asm volatile("s_waitcnt lgkmcnt(%0)" ::"n"(n) : "memory")

constexpr int PK1_CHUNKS = 27 * 10 * 2 * 64 * 8, PK2_CHUNKS = 8 * 27 * 64 * 10;
constexpr int PK_WGS = (PK1_CHUNKS + PK2_CHUNKS) / 1024;
__device__ __forceinline__ void pack_weights(int wgi, const u16* __restrict__ wg, const u16* __restrict__ wu,
                                             const u16* __restrict__ wd, u16* __restrict__ wpk1, u16* __restrict__ wpk2) {
  u32x4 v[4];
#pragma unroll
  for (int i = 0; i < 4; ++i) {
    const int c = wgi * 1024 + i * 256 + threadIdx.x;
    if (c < PK1_CHUNKS) {
      const int cp = c & 7, k = (c >> 3) & 63, gu = (c >> 9) & 1, tk = c >> 10;
      const int nt = tk / 10, kt = tk - nt * 10;
      const int col = nt * 64 + (((cp >> 2) ^ ((k >> 1) & 1)) * 32) + (cp & 3) * 8;
      v[i] = *(const u32x4*)((gu ? wu : wg) + (size_t)(kt * 64 + k) * F + col);
    } else {
      const int c2 = c - PK1_CHUNKS;
      const int rk = c2 / 10, ch = c2 - rk * 10;
      const int tk = rk >> 6, k = rk & 63;
      const int nt = tk / 27, kt = tk - nt * 27;
      v[i] = *(const u32x4*)(wd + (size_t)(kt * 64 + k) * D + nt * 80 + ch * 8);
    }
  }
#pragma unroll
  for (int i = 0; i < 4; ++i) {
    const int c = wgi * 1024 + i * 256 + threadIdx.x;
    if (c < PK1_CHUNKS) *(u32x4*)(wpk1 + (size_t)c * 8) = v[i];
    else *(u32x4*)(wpk2 + (size_t)(c - PK1_CHUNKS) * 8) = v[i];
  }
}

__global__ __launch_bounds__(256) void k_prep(const u16* __restrict__ k, const u16* __restrict__ v,
                                              const u16* __restrict__ shift_k, u16* __restrict__ kvT,
                                              const u16* __restrict__ wg, const u16* __restrict__ wu,
                                              const u16* __restrict__ wd, u16* __restrict__ wpk1, u16* __restrict__ wpk2,
                                              unsigned* __restrict__ cnt) {
  __shared__ __attribute__((aligned(16))) unsigned tile[64][36];
  if (blockIdx.x == 0 && threadIdx.x < 64) cnt[threadIdx.x * 64] = 0u;
  if (blockIdx.x >= 320) { pack_weights(blockIdx.x - 320, wg, wu, wd, wpk1, wpk2); return; }
  const int t0 = (blockIdx.x & 31) * 64, d0 = (blockIdx.x >> 5) * 64;
  const int tid = threadIdx.x;
  const int cd = (tid & 7) * 8, tp = tid >> 3;
  const int t = t0 + 2 * tp;
  u32x4 kk[5], vv[2], sw[4];
#pragma unroll
  for (int j = 0; j < 5; ++j) {
    u32x4 z = {0u, 0u, 0u, 0u};
    kk[j] = (t - 3 + j >= 0) ? *(const u32x4*)(k + (size_t)(t - 3 + j) * D + d0 + cd) : z;
  }
  vv[0] = *(const u32x4*)(v + (size_t)t * D + d0 + cd);
  vv[1] = *(const u32x4*)(v + (size_t)(t + 1) * D + d0 + cd);
#pragma unroll
  for (int i = 0; i < 4; ++i) sw[i] = *(const u32x4*)(shift_k + (size_t)(d0 + cd) * 4 + 8 * i);
#pragma unroll
  for (int cp = 0; cp < 4; ++cp) {
    f32x2 wj[4];
    wj[0] = (f32x2){bflo(sw[cp][0]), bflo(sw[cp][2])};
    wj[1] = (f32x2){bfhi(sw[cp][0]), bfhi(sw[cp][2])};
    wj[2] = (f32x2){bflo(sw[cp][1]), bflo(sw[cp][3])};
    wj[3] = (f32x2){bfhi(sw[cp][1]), bfhi(sw[cp][3])};
    f32x2 kf[5];
#pragma unroll
    for (int j = 0; j < 5; ++j) kf[j] = unpk(kk[j][cp]);
    unsigned o[2];
#pragma unroll
    for (int rr = 0; rr < 2; ++rr) {
      f32x2 ks = rbf2(kf[rr + 0] * wj[3]);
      ks = rbf2(ks + rbf2(kf[rr + 1] * wj[2]));
      ks = rbf2(ks + rbf2(kf[rr + 2] * wj[1]));
      ks = rbf2(ks + rbf2(kf[rr + 3] * wj[0]));
      o[rr] = cvtpk(ks * unpk(vv[rr][cp]));
    }
    tile[cd + 2 * cp][tp] = (o[0] & 0xffffu) | (o[1] << 16);
    tile[cd + 2 * cp + 1][tp] = (o[0] >> 16) | (o[1] & 0xffff0000u);
  }
  __syncthreads();
  {
    const int dl = tid >> 2, c = (tid & 3) * 8;
    u32x4 a = *(const u32x4*)&tile[dl][c];
    u32x4 b = *(const u32x4*)&tile[dl][c + 4];
    u16* dst = kvT + (size_t)(d0 + dl) * S + t0 + 2 * c;
    *(u32x4*)dst = a;
    *(u32x4*)(dst + 8) = b;
  }
}

constexpr int CV_T_BYTES = 2112 * 8;
constexpr int CV_U_OFF = 32768;
constexpr int CV_U_ROW = 144;
constexpr int CV_LDS = CV_U_OFF + 64 * CV_U_ROW;

__global__ __launch_bounds__(256) void k_conv(const u16* __restrict__ kvT, const u16* __restrict__ ssm_k,
                                              const u16* __restrict__ ssm_D, u16* __restrict__ YT) {
  __shared__ __attribute__((aligned(16))) char lds[CV_LDS];
  const int d = blockIdx.x;
  const int tid = threadIdx.x, lane = tid & 63, w = tid >> 6;
  const u16* h = ssm_k + (size_t)d * S;
  const u16* u = kvT + (size_t)d * S;
  {
    const int base = 2040 - 8 * tid;
    u32x4 hi = *(const u32x4*)(h + base);
    u32x4 z = {0u, 0u, 0u, 0u};
    u32x4 lo = (base >= 8) ? *(const u32x4*)(h + base - 8) : z;
    unsigned Dw[8] = {lo[0], lo[1], lo[2], lo[3], hi[0], hi[1], hi[2], hi[3]};
    unsigned ent[16];
#pragma unroll
    for (int c = 0; c < 4; ++c) {
      ent[4 * c + 0] = __builtin_amdgcn_alignbit(Dw[7 - c], Dw[7 - c], 16);
      ent[4 * c + 1] = __builtin_amdgcn_alignbit(Dw[6 - c], Dw[6 - c], 16);
      ent[4 * c + 2] = (Dw[7 - c] & 0xffffu) | (Dw[6 - c] & 0xffff0000u);
      ent[4 * c + 3] = (Dw[6 - c] & 0xffffu) | (Dw[5 - c] & 0xffff0000u);
    }
    u32x4* tp = (u32x4*)(lds + tid * 64);
#pragma unroll
    for (int c = 0; c < 4; ++c) {
      u32x4 q = {ent[4 * c], ent[4 * c + 1], ent[4 * c + 2], ent[4 * c + 3]};
      tp[c] = q;
    }
    if (tid < 32) *(u32x4*)(lds + 2048 * 8 + tid * 16) = z;
    u32x4 uu = *(const u32x4*)(u + tid * 8);
    *(u32x4*)(lds + CV_U_OFF + (32 + (tid >> 3)) * CV_U_ROW + (tid & 7) * 16) = uu;
    for (int i = tid; i < 32 * CV_U_ROW / 16; i += 256) *(u32x4*)(lds + CV_U_OFF + i * 16) = z;
  }
  __syncthreads();
  const int r = lane & 31, hh = lane >> 5;
  f32x16 acc0 = {}, acc1 = {};
  {
    const char* tb = lds + (2047 - r + 8 * hh) * 8;
    const char* ub = lds + CV_U_OFF + (r + 32) * CV_U_ROW + hh * 16;
    auto ldT = [&](int q) -> s16x8 {
      const char* p = tb - q * 128;
      s16x4 a = *(const s16x4*)p;
      s16x4 b = *(const s16x4*)(p + 32);
      return __builtin_shufflevector(a, b, 0, 1, 2, 3, 4, 5, 6, 7);
    };
    const int m0 = 8 * w;
    s16x8 Tm3 = ldT(4 * m0 - 3), Tm2 = ldT(4 * m0 - 2);
#pragma unroll 2
    for (int mm = 0; mm < 8; ++mm) {
      const int m = m0 + mm;
      s16x8 Tm1 = ldT(4 * m - 1), T0 = ldT(4 * m), T1 = ldT(4 * m + 1), T2 = ldT(4 * m + 2);
      const char* up = ub - m * CV_U_ROW;
      s16x8 U0 = *(const s16x8*)(up), U1 = *(const s16x8*)(up + 32), U2 = *(const s16x8*)(up + 64), U3 = *(const s16x8*)(up + 96);
      acc0 = __builtin_amdgcn_mfma_f32_32x32x16_bf16(U0, T0, acc0, 0, 0, 0);
      acc1 = __builtin_amdgcn_mfma_f32_32x32x16_bf16(U0, T2, acc1, 0, 0, 0);
      acc0 = __builtin_amdgcn_mfma_f32_32x32x16_bf16(U1, Tm1, acc0, 0, 0, 0);
      acc1 = __builtin_amdgcn_mfma_f32_32x32x16_bf16(U1, T1, acc1, 0, 0, 0);
      acc0 = __builtin_amdgcn_mfma_f32_32x32x16_bf16(U2, Tm2, acc0, 0, 0, 0);
      acc1 = __builtin_amdgcn_mfma_f32_32x32x16_bf16(U2, T0, acc1, 0, 0, 0);
      acc0 = __builtin_amdgcn_mfma_f32_32x32x16_bf16(U3, Tm3, acc0, 0, 0, 0);
      acc1 = __builtin_amdgcn_mfma_f32_32x32x16_bf16(U3, Tm1, acc1, 0, 0, 0);
      Tm3 = T1; Tm2 = T2;
    }
  }
  asm volatile("" :: "v"(acc0[0]), "v"(acc1[0]));
  __syncthreads();
  {
    float* P = (float*)lds + w * 2048;
#pragma unroll
    for (int reg = 0; reg < 16; ++reg) {
      const int i = (reg & 3) + 8 * (reg >> 2) + 4 * hh;
      P[64 * i + r] = acc0[reg];
      P[64 * i + 32 + r] = acc1[reg];
    }
  }
  __syncthreads();
  {
    const float* P = (const float*)lds + tid * 8;
    float s[8];
#pragma unroll
    for (int e = 0; e < 8; ++e) s[e] = 0.f;
#pragma unroll
    for (int ww = 0; ww < 4; ++ww) {
      f32x4 a = *(const f32x4*)(P + ww * 2048), b = *(const f32x4*)(P + ww * 2048 + 4);
#pragma unroll
      for (int e = 0; e < 4; ++e) { s[e] += a[e]; s[4 + e] += b[e]; }
    }
    u32x4 uu = *(const u32x4*)(lds + CV_U_OFF + (32 + (tid >> 3)) * CV_U_ROW + (tid & 7) * 16);
    const float Dv = bf2f(ssm_D[d]);
    u32x4 o;
#pragma unroll
    for (int e = 0; e < 4; ++e) {
      float y0 = rbf(s[2 * e]) + rbf(bflo(uu[e]) * Dv);
      float y1 = rbf(s[2 * e + 1]) + rbf(bfhi(uu[e]) * Dv);
      o[e] = pack2(y0, y1);
    }
    *(u32x4*)(YT + (size_t)d * S + tid * 8) = o;
  }
}

template <int CTRL> __device__ __forceinline__ float dpp_add(float v) {
  return v + __builtin_bit_cast(float, __builtin_amdgcn_update_dpp(0, __builtin_bit_cast(int, v), CTRL, 0xf, 0xf, true));
}
__device__ __forceinline__ float wave_sum(float v) {
  v = dpp_add<0xB1>(v);
  v = dpp_add<0x4E>(v);
  v = dpp_add<0x141>(v);
  v = dpp_add<0x140>(v);
  const int iv = __builtin_bit_cast(int, v);
  const float a = __builtin_bit_cast(float, __builtin_amdgcn_readlane(iv, 0));
  const float b = __builtin_bit_cast(float, __builtin_amdgcn_readlane(iv, 16));
  const float c = __builtin_bit_cast(float, __builtin_amdgcn_readlane(iv, 32));
  const float d = __builtin_bit_cast(float, __builtin_amdgcn_readlane(iv, 48));
  return (a + b) + (c + d);
}
__device__ __forceinline__ void ln_row(const f32x2 (&x)[5], unsigned (&o)[5]) {
  f32x2 s2 = (x[0] + x[1]) + (x[2] + x[3]) + x[4];
  const float mean = wave_sum(s2[0] + s2[1]) * (1.0f / D);
  f32x2 q2 = {0.f, 0.f};
#pragma unroll
  for (int i = 0; i < 5; ++i) { f32x2 dd = x[i] - mean; q2 += dd * dd; }
  const float var = rbf(wave_sum(q2[0] + q2[1]) * (1.0f / D));
  const float mb = rbf(mean);
  const float sd = rbf(__builtin_amdgcn_sqrtf(rbf(var + 1e-5f)));
  const float rs = __builtin_amdgcn_rcpf(sd);
#pragma unroll
  for (int i = 0; i < 5; ++i) o[i] = cvtpk(rbf2(x[i] - mb) * rs);
}

__device__ __forceinline__ unsigned lds_addr(const void* p) {
  return (unsigned)(uintptr_t)(const __attribute__((address_space(3))) char*)p;
}
template <bool SC1 = false>
__device__ __forceinline__ void glds16(const void* sbase, unsigned voff, unsigned ldst) {
  const unsigned long long pv = (unsigned long long)sbase;
  const unsigned long long ub = ((unsigned long long)(unsigned)__builtin_amdgcn_readfirstlane((unsigned)(pv >> 32)) << 32) |
                                (unsigned)__builtin_amdgcn_readfirstlane((unsigned)pv);
  const unsigned ul = __builtin_amdgcn_readfirstlane(ldst);
  unsigned keep;
  if (SC1)
    asm volatile("s_mov_b32 %0, m0\n\ts_mov_b32 m0, %3\n\ts_nop 0\n\tglobal_load_lds_dwordx4 %1, %2 sc1\n\ts_mov_b32 m0, %0"
                 : "=&s"(keep) : "v"(voff), "s"(ub), "s"(ul) : "memory");
  else
    asm volatile("s_mov_b32 %0, m0\n\ts_mov_b32 m0, %3\n\ts_nop 0\n\tglobal_load_lds_dwordx4 %1, %2\n\ts_mov_b32 m0, %0"
                 : "=&s"(keep) : "v"(voff), "s"(ub), "s"(ul) : "memory");
}
__device__ __forceinline__ s16x8 tr_frag(const char* p0, const char* p1) {
  s16x4 a = __builtin_amdgcn_ds_read_tr16_b64_v4i16((lds_s16x4_ptr)p0);
  s16x4 b = __builtin_amdgcn_ds_read_tr16_b64_v4i16((lds_s16x4_ptr)p1);
  return __builtin_shufflevector(a, b, 0, 1, 2, 3, 4, 5, 6, 7);
}

#define RLX_AGENT __ATOMIC_RELAXED, __HIP_MEMORY_SCOPE_AGENT
__device__ __forceinline__ void st16_sc1(void* p, u32x4 v) { asm volatile("global_store_dwordx4 %0, %1, off sc1\n\ts_nop 1" ::"v"(p), "v"(v) : "memory"); }
__device__ __forceinline__ void st8_sc1(void* p, u32x2 v) { asm volatile("global_store_dwordx2 %0, %1, off sc1\n\ts_nop 1" ::"v"(p), "v"(v) : "memory"); }
__device__ __forceinline__ void wg_signal(unsigned* cnt) {
  asm volatile("s_waitcnt vmcnt(0) lgkmcnt(0)" ::: "memory");
  __builtin_amdgcn_s_barrier();
  if (threadIdx.x == 0) __hip_atomic_fetch_add(cnt, 1u, RLX_AGENT);
}
__device__ __forceinline__ void wg_wait(unsigned* cnt, unsigned want) {
  if (threadIdx.x < 64) {
    unsigned spins = 0;
    while ((unsigned)__builtin_amdgcn_readfirstlane(__hip_atomic_load(cnt, RLX_AGENT)) < want) {
      __builtin_amdgcn_s_sleep(1);
      if (++spins > (1u << 22)) break;
    }
  }
  asm volatile("" ::: "memory");
  __builtin_amdgcn_s_barrier();
  asm volatile("" ::: "memory");
}

#define SCHED() __builtin_amdgcn_sched_barrier(0)
#define ILV_MFMA_DS()                                        \
  do {                                                       \
    __builtin_amdgcn_sched_group_barrier(0x008, 1, 0);       \
    __builtin_amdgcn_sched_group_barrier(0x100, 2, 0);       \
    __builtin_amdgcn_sched_group_barrier(0x008, 1, 0);       \
    __builtin_amdgcn_sched_group_barrier(0x100, 2, 0);       \
    __builtin_amdgcn_sched_group_barrier(0x008, 1, 0);       \
    __builtin_amdgcn_sched_group_barrier(0x100, 2, 0);       \
    __builtin_amdgcn_sched_group_barrier(0x008, 1, 0);       \
  } while (0)

__device__ __forceinline__ unsigned quad_get(unsigned v, int sel) {
  switch (sel) {
    case 1: return __builtin_amdgcn_update_dpp(0, (int)v, 0x55, 0xf, 0xf, true);
    case 2: return __builtin_amdgcn_update_dpp(0, (int)v, 0xAA, 0xf, 0xf, true);
    default: return __builtin_amdgcn_update_dpp(0, (int)v, 0xFF, 0xf, 0xf, true);
  }
}
template <int MODE>
__device__ __forceinline__ void row_phase(char* lds, int t0, int w, int lane, const u16* __restrict__ xin,
                                          const u16* __restrict__ YT, u16* __restrict__ Y,
                                          u16* __restrict__ xout, u16* __restrict__ xn) {
  u16 (*yt)[D + 8] = (u16 (*)[D + 8])lds;
  const int t = t0 + (w & 7);
  u32x4 xa = {0u, 0u, 0u, 0u}; unsigned xb = 0u;
  if (w < 8) {
    if (MODE == 0) {
      xa = *(const u32x4*)(xin + (size_t)t * D + 8 * lane);
      xb = *(const unsigned*)(xin + (size_t)t * D + 512 + 2 * lane);
    } else {
      const unsigned long long* xp = (const unsigned long long*)(xin + (size_t)t * D + 8 * lane);
      const unsigned long long v0 = __hip_atomic_load(xp, RLX_AGENT), v1 = __hip_atomic_load(xp + 1, RLX_AGENT);
      xa = (u32x4){(unsigned)v0, (unsigned)(v0 >> 32), (unsigned)v1, (unsigned)(v1 >> 32)};
      xb = __hip_atomic_load((const unsigned*)(xin + (size_t)t * D + 512 + 2 * lane), RLX_AGENT);
    }
  }
  u32x4 ya; unsigned yb;
  if (MODE == 0) {
    const int dd = w * 64 + lane;
    if (dd < D) {
      u32x4 q = *(const u32x4*)(YT + (size_t)dd * S + t0);
#pragma unroll
      for (int e = 0; e < 4; ++e) {
        yt[2 * e][dd] = (u16)(q[e] & 0xffffu);
        yt[2 * e + 1][dd] = (u16)(q[e] >> 16);
      }
    }
    __syncthreads();
  }
  if (w < 8) {
    if (MODE == 0) {
      ya = *(const u32x4*)&yt[w][8 * lane];
      yb = *(const unsigned*)&yt[w][512 + 2 * lane];
      *(u32x4*)(Y + (size_t)t * D + 8 * lane) = ya;
      *(unsigned*)(Y + (size_t)t * D + 512 + 2 * lane) = yb;
    } else {
      ya = *(const u32x4*)(Y + (size_t)t * D + 8 * lane);
      yb = *(const unsigned*)(Y + (size_t)t * D + 512 + 2 * lane);
    }
    f32x2 x[5] = {unpk(xa[0]), unpk(xa[1]), unpk(xa[2]), unpk(xa[3]), unpk(xb)};
    const unsigned yp[5] = {ya[0], ya[1], ya[2], ya[3], yb};
    unsigned n[5], xo[5];
    ln_row(x, n);
#pragma unroll
    for (int i = 0; i < 5; ++i) { xo[i] = cvtpk(x[i] + rbf2(unpk(n[i]) * unpk(yp[i]))); x[i] = unpk(xo[i]); }
    ln_row(x, n);
    const u32x4 xt = {xo[4], quad_get(xo[4], 1), quad_get(xo[4], 2), quad_get(xo[4], 3)};
    const u32x4 nt4 = {n[4], quad_get(n[4], 1), quad_get(n[4], 2), quad_get(n[4], 3)};
    const int sw = (t >> 1) & 7;
    u16* bp = xn + (size_t)(t >> 8) * (10 * 256 * 64) + (t & 255) * 64;
    st16_sc1(xout + (size_t)t * D + 8 * lane, (u32x4){xo[0], xo[1], xo[2], xo[3]});
    st16_sc1(bp + (lane >> 3) * (256 * 64) + (((lane & 7) ^ sw) * 8), (u32x4){n[0], n[1], n[2], n[3]});
    if ((lane & 3) == 0) {
      st16_sc1(xout + (size_t)t * D + 512 + 2 * lane, xt);
      st16_sc1(bp + (8 + (lane >> 5)) * (256 * 64) + ((((lane & 31) >> 2) ^ sw) * 8), nt4);
    }
  }
}

constexpr int G1_A = 256 * 64 * 2, G1_B = 64 * 128 * 2, G1_ST = G1_A + G1_B, G1_NS = 3;

__device__ __forceinline__ void gemm1_pre(char* lds, int nt, int w, int lane, const u16* __restrict__ wpk) {
  if (w >= 8) {
    const int lw = w - 8;
    const unsigned lbase = lds_addr(lds);
    const char* bbase = (const char*)wpk + (size_t)nt * 10 * 16384 + lw * 4096;
#pragma unroll
    for (int st = 0; st < 3; ++st)
#pragma unroll
      for (int i = 0; i < 4; ++i) glds16(bbase + st * 16384 + i * 1024, lane * 16, lbase + st * G1_ST + G1_A + (lw * 4 + i) * 1024);
  }
}
__device__ __forceinline__ void gemm1_phase(char* lds, int mt, int nt, int w, int lane, const u16* __restrict__ xn,
                                            const u16* __restrict__ wpk, u16* __restrict__ hout) {
  constexpr int NT = D / 64;
  if (w >= 8) {
    const int lw = w - 8;
    const unsigned lbase = lds_addr(lds);
    const unsigned voff = lane * 16;
    const char* abase = (const char*)xn + (size_t)mt * 10 * 32768 + lw * 8192;
    const char* bbase = (const char*)wpk + (size_t)nt * 10 * 16384 + lw * 4096;
    auto stageA = [&](int buf, int kt) {
#pragma unroll
      for (int i = 0; i < 8; ++i) glds16<true>(abase + kt * 32768 + i * 1024, voff, lbase + buf * G1_ST + (lw * 8 + i) * 1024);
    };
    auto stageB = [&](int buf, int kt) {
#pragma unroll
      for (int i = 0; i < 4; ++i) glds16(bbase + kt * 16384 + i * 1024, voff, lbase + buf * G1_ST + G1_A + (lw * 4 + i) * 1024);
    };
    stageA(0, 0);
    stageA(1, 1);
    WAIT_V(8);
    __builtin_amdgcn_s_barrier();
    stageA(2, 2);
    WAIT_V(8);
    __builtin_amdgcn_s_barrier();
    stageB(0, 3); stageA(0, 3);
    int buf = 1;
#pragma unroll 1
    for (int kt = 1; kt + 3 < NT; ++kt) {
      WAIT_V(12);
      __builtin_amdgcn_s_barrier();
      stageB(buf, kt + 3); stageA(buf, kt + 3);
      buf = (buf == 2) ? 0 : buf + 1;
    }
    WAIT_V(12);
    __builtin_amdgcn_s_barrier();
    WAIT_V(0);
    __builtin_amdgcn_s_barrier();
  } else {
    const int wm = w >> 1, wn = w & 1;
    f32x16 ag[2] = {}, au[2] = {};
    const int r = lane & 31, hh = lane >> 5;
    const int arow = (wm * 64 + r) * 128;
    const int asw = (r >> 1) & 7;
    const int blk = (lane >> 4) & 1, q = (lane & 15) >> 2, p = lane & 3;
    const int boff = G1_A + (8 * hh + q) * 128 + ((wn ^ ((q >> 1) & 1)) * 64) + blk * 32 + p * 8;
    struct Frag { s16x8 a0, a1, bg, bu; };
    auto ldfrag = [&](const char* base, int ks) -> Frag {
      Frag f;
      const char* ap = base + arow + (((2 * ks + hh) ^ asw) * 16);
      const char* bp = base + boff + ks * 2048;
      f.bg = tr_frag(bp, bp + 512);
      f.a0 = *(const s16x8*)ap;
      f.bu = tr_frag(bp + 8192, bp + 8192 + 512);
      f.a1 = *(const s16x8*)(ap + 4096);
      return f;
    };
    auto mma = [&](const Frag& f) {
      ag[0] = __builtin_amdgcn_mfma_f32_32x32x16_bf16(f.bg, f.a0, ag[0], 0, 0, 0);
      au[0] = __builtin_amdgcn_mfma_f32_32x32x16_bf16(f.bu, f.a0, au[0], 0, 0, 0);
      ag[1] = __builtin_amdgcn_mfma_f32_32x32x16_bf16(f.bg, f.a1, ag[1], 0, 0, 0);
      au[1] = __builtin_amdgcn_mfma_f32_32x32x16_bf16(f.bu, f.a1, au[1], 0, 0, 0);
    };
    asm volatile("" ::: "memory");
    __builtin_amdgcn_s_barrier();
    asm volatile("" ::: "memory");
    Frag f0_ = ldfrag(lds, 0);
    int buf = 0;
#pragma unroll 1
    for (int kt = 0; kt < NT - 1; ++kt) {
      const char* base = lds + buf * G1_ST;
      SCHED();
      Frag f1_ = ldfrag(base, 1);
      mma(f0_);
      ILV_MFMA_DS();
      SCHED();
      Frag f2_ = ldfrag(base, 2);
      mma(f1_);
      ILV_MFMA_DS();
      SCHED();
      Frag f3_ = ldfrag(base, 3);
      mma(f2_);
      ILV_MFMA_DS();
      SCHED();
      buf = (buf == 2) ? 0 : buf + 1;
      WAIT_L(0);
      __builtin_amdgcn_s_barrier();
      asm volatile("" ::: "memory");
      SCHED();
      f0_ = ldfrag(lds + buf * G1_ST, 0);
      mma(f3_);
      ILV_MFMA_DS();
      SCHED();
    }
    {
      const char* base = lds + buf * G1_ST;
      Frag f1_ = ldfrag(base, 1);
      mma(f0_);
      ILV_MFMA_DS();
      SCHED();
      Frag f2_ = ldfrag(base, 2);
      mma(f1_);
      ILV_MFMA_DS();
      SCHED();
      Frag f3_ = ldfrag(base, 3);
      mma(f2_);
      ILV_MFMA_DS();
      SCHED();
      mma(f3_);
    }
    constexpr int HROW = 80;
    char* slab = lds + G1_ST + w * (64 * HROW);
    u16* hbase = hout + ((size_t)(mt * 4 + wm) * 27 + nt) * 4096;
#pragma unroll
    for (int mi = 0; mi < 2; ++mi) {
      char* hp = slab + (mi * 32 + r) * HROW + 8 * hh;
#pragma unroll
      for (int gq = 0; gq < 4; ++gq) {
        u32x2 o;
#pragma unroll
        for (int e = 0; e < 2; ++e) {
          const f32x2 gv = rbf2((f32x2){ag[mi][4 * gq + 2 * e], ag[mi][4 * gq + 2 * e + 1]}) * -1.44269504f;
          const f32x2 uv = rbf2((f32x2){au[mi][4 * gq + 2 * e], au[mi][4 * gq + 2 * e + 1]});
          const f32x2 den = (f32x2){__builtin_amdgcn_exp2f(gv[0]), __builtin_amdgcn_exp2f(gv[1])} + 1.0f;
          const f32x2 sg = rbf2((f32x2){__builtin_amdgcn_rcpf(den[0]), __builtin_amdgcn_rcpf(den[1])});
          o[e] = cvtpk(sg * uv);
        }
        *(u32x2*)(hp + 16 * gq) = o;
      }
      asm volatile("s_waitcnt lgkmcnt(0)" ::: "memory");
#pragma unroll
      for (int i = 0; i < 2; ++i) {
        const int row = mi * 32 + i * 16 + (lane >> 2), c = lane & 3;
        u32x4 o = *(const u32x4*)(slab + row * HROW + c * 16);
        st16_sc1(hbase + row * 64 + (((wn * 4 + c) ^ ((row >> 1) & 7)) * 8), o);
      }
    }
  }
}

constexpr int G2_A = 64 * 64 * 2, G2_B = 64 * 80 * 2, G2_ST = G2_A + G2_B, G2_NS = 6, G2_LAG = 1;

__device__ __forceinline__ void gemm2_pre(char* lds, int nt, int w, int lane, const u16* __restrict__ wpk) {
  if (w >= 4) {
    const int lw = w - 4;
    const unsigned lbase = lds_addr(lds);
    const char* bbase = (const char*)wpk + (size_t)nt * 27 * 10240 + lw * 1024;
#pragma unroll
    for (int st = 0; st < G2_NS; ++st) {
      glds16(bbase + st * 10240, lane * 16, lbase + st * G2_ST + G2_A + lw * 1024);
      if (lw < 2) glds16(bbase + st * 10240 + 8192, lane * 16, lbase + st * G2_ST + G2_A + (lw + 8) * 1024);
    }
  }
}
template <bool HANDOFF>
__device__ __forceinline__ void gemm2_phase(char* lds, int mt, int nt, int w, int lane, const u16* __restrict__ hin,
                                            const u16* __restrict__ wpk, const u16* __restrict__ xres,
                                            u16* __restrict__ out) {
  constexpr int NT = F / 64;
  const int m0 = mt * 64, n0 = nt * 80;
  if (w >= 4) {
    const int lw = w - 4;
    const unsigned lbase = lds_addr(lds);
    const unsigned voff = lane * 16;
    const char* abase = (const char*)hin + (size_t)mt * 27 * 8192 + lw * 1024;
    const char* bbase = (const char*)wpk + (size_t)nt * 27 * 10240 + lw * 1024;
    auto stageA = [&](int buf, int kt) { glds16<true>(abase + kt * 8192, voff, lbase + buf * G2_ST + lw * 1024); };
    auto stage = [&](int buf, int kt) {
      const unsigned a = lbase + buf * G2_ST;
      glds16<true>(abase + kt * 8192, voff, a + lw * 1024);
      glds16(bbase + kt * 10240, voff, a + G2_A + lw * 1024);
      if (lw < 2) glds16(bbase + kt * 10240 + 8192, voff, a + G2_A + (lw + 8) * 1024);
    };
    stageA(0, 0);
    stageA(1, 1);
    WAIT_V(1);
    __builtin_amdgcn_s_barrier();
#pragma unroll
    for (int i = 2; i < G2_NS; ++i) stageA(i, i);
    auto loader_loop = [&](auto per_tile_c) {
      constexpr int PT = decltype(per_tile_c)::value;
      WAIT_V(4); __builtin_amdgcn_s_barrier();
      WAIT_V(3); __builtin_amdgcn_s_barrier(); stage(0, 6);
      WAIT_V(2 + PT); __builtin_amdgcn_s_barrier(); stage(1, 7);
      WAIT_V(1 + 2 * PT); __builtin_amdgcn_s_barrier(); stage(2, 8);
      int buf = 3;
#pragma unroll 1
      for (int kt = 4; kt - 1 + G2_NS < NT; ++kt) {
        WAIT_V(PT * 3);
        __builtin_amdgcn_s_barrier();
        stage(buf, kt - 1 + G2_NS);
        buf = (buf == G2_NS - 1) ? 0 : buf + 1;
      }
#pragma unroll
      for (int i = G2_NS - 2; i >= 1; --i) { WAIT_V(PT * (i - 1)); __builtin_amdgcn_s_barrier(); }
    };
    if (lw < 2) loader_loop(std::integral_constant<int, 3>{}); else loader_loop(std::integral_constant<int, 2>{});
    __builtin_amdgcn_s_barrier();
    __builtin_amdgcn_s_barrier();
  } else {
    const int wm = w & 1, kh = w >> 1;
    f32x4 acc[2][5] = {};
    const int r = lane & 15, g4 = lane >> 4;
    const int aoff0 = (wm * 32 + r) * 128 + (((4 * kh + g4) ^ ((r >> 1) & 7)) * 16);
    const int q = (lane & 15) >> 2, p = lane & 3;
    const int boff = G2_A + (32 * kh + 8 * g4 + q) * 160 + p * 8;
    struct Frag { s16x8 a[2], b[5]; };
    auto ldfrag = [&](const char* base) -> Frag {
      Frag f;
      f.a[0] = *(const s16x8*)(base + aoff0);
      f.a[1] = *(const s16x8*)(base + aoff0 + 2048);
#pragma unroll
      for (int c = 0; c < 5; ++c) f.b[c] = tr_frag(base + boff + c * 32, base + boff + c * 32 + 640);
      return f;
    };
    auto mma = [&](const Frag& f) {
#pragma unroll
      for (int c = 0; c < 5; ++c) {
        acc[0][c] = __builtin_amdgcn_mfma_f32_16x16x32_bf16(f.b[c], f.a[0], acc[0][c], 0, 0, 0);
        acc[1][c] = __builtin_amdgcn_mfma_f32_16x16x32_bf16(f.b[c], f.a[1], acc[1][c], 0, 0, 0);
      }
    };
    u32x2 xr[2][5];
    if (kh == 0) {
#pragma unroll
      for (int mi = 0; mi < 2; ++mi)
#pragma unroll
        for (int c = 0; c < 5; ++c) {
          const unsigned long long v = __hip_atomic_load((const unsigned long long*)(xres + (size_t)(m0 + wm * 32 + mi * 16 + r) * D + n0 + c * 16 + 4 * g4), RLX_AGENT);
          xr[mi][c] = (u32x2){(unsigned)v, (unsigned)(v >> 32)};
        }
    }
    asm volatile("" ::: "memory");
    __builtin_amdgcn_s_barrier();
    asm volatile("" ::: "memory");
    Frag fa = ldfrag(lds);
    int buf = 0;
#pragma unroll 1
    for (int kt = 0; kt < NT - 1; kt += 2) {
      buf = (buf == G2_NS - 1) ? 0 : buf + 1;
      __builtin_amdgcn_s_barrier();
      asm volatile("" ::: "memory");
      SCHED();
      Frag fb = ldfrag(lds + buf * G2_ST);
      mma(fa);
#pragma unroll
      for (int i = 0; i < 6; ++i) { __builtin_amdgcn_sched_group_barrier(0x008, 1, 0); __builtin_amdgcn_sched_group_barrier(0x100, 2, 0); }
      __builtin_amdgcn_sched_group_barrier(0x008, 4, 0);
      SCHED();
      buf = (buf == G2_NS - 1) ? 0 : buf + 1;
      __builtin_amdgcn_s_barrier();
      asm volatile("" ::: "memory");
      SCHED();
      fa = ldfrag(lds + buf * G2_ST);
      mma(fb);
#pragma unroll
      for (int i = 0; i < 6; ++i) { __builtin_amdgcn_sched_group_barrier(0x008, 1, 0); __builtin_amdgcn_sched_group_barrier(0x100, 2, 0); }
      __builtin_amdgcn_sched_group_barrier(0x008, 4, 0);
      SCHED();
    }
    mma(fa);
    WAIT_L(0);
    __builtin_amdgcn_s_barrier();
    asm volatile("" ::: "memory");
    constexpr int PROW = 336;
    if (kh == 1) {
#pragma unroll
      for (int mi = 0; mi < 2; ++mi)
#pragma unroll
        for (int c = 0; c < 5; ++c)
          *(f32x4*)(lds + (wm * 32 + mi * 16 + r) * PROW + (c * 16 + 4 * g4) * 4) = acc[mi][c];
    }
    WAIT_L(0);
    __builtin_amdgcn_s_barrier();
    asm volatile("" ::: "memory");
    if (kh == 0) {
#pragma unroll
      for (int mi = 0; mi < 2; ++mi) {
        const int row = m0 + wm * 32 + mi * 16 + r;
#pragma unroll
        for (int c = 0; c < 5; ++c) {
          f32x4 o = *(const f32x4*)(lds + (wm * 32 + mi * 16 + r) * PROW + (c * 16 + 4 * g4) * 4);
          const size_t idx = (size_t)row * D + n0 + c * 16 + 4 * g4;
          const u32x2 xv = xr[mi][c];
          u32x2 ov = {cvtpk(unpk(xv[0]) + rbf2((f32x2){acc[mi][c][0] + o[0], acc[mi][c][1] + o[1]})),
                      cvtpk(unpk(xv[1]) + rbf2((f32x2){acc[mi][c][2] + o[2], acc[mi][c][3] + o[3]}))};
          if (HANDOFF) st8_sc1(out + idx, ov); else *(u32x2*)(out + idx) = ov;
        }
      }
    }
  }
}

constexpr int CNT_STRIDE = 64;
constexpr int CNT_A = 0, CNT_B = 8, CNT_C = 16, CNT_D = 48, CNT_E = 56, CNT_N = 64;

struct MainArgs {
  const u16* q; const u16* YT; u16* Y; u16* x1; u16* x2; u16* x3; u16* xn1; u16* xn2; u16* h1; u16* h2;
  const u16* wpk1; const u16* wpk2; u16* out; unsigned* cnt;
};

__global__ __launch_bounds__(768) void k_main(MainArgs a) {
  __shared__ __attribute__((aligned(1024))) char lds[G1_NS * G1_ST];
  const int id = blockIdx.x;
  const int g = id & 7, j = id >> 3;
  const int tid = threadIdx.x, lane = tid & 63;
  const int w = __builtin_amdgcn_readfirstlane(tid >> 6);
  const int p64 = g * 4 + (j & 3), n2 = j >> 2;
  const int t0 = p64 * 64 + n2 * 8;
  const int pm = p64 >> 2;
  const int mt1 = (g & 1) * 4 + (j & 3), nj = j >> 2, nt1 = (g >> 1) * 7 + nj;
  const bool has1 = (nj < 7) && (nt1 < 27);
  unsigned* cnt = a.cnt;

  row_phase<0>(lds, t0, w, lane, a.q, a.YT, a.Y, a.x1, a.xn1);
  wg_signal(cnt + (CNT_A + pm) * CNT_STRIDE);
  if (has1) {
    gemm1_pre(lds, nt1, w, lane, a.wpk1);
    wg_wait(cnt + (CNT_A + mt1) * CNT_STRIDE, 32);
    gemm1_phase(lds, mt1, nt1, w, lane, a.xn1, a.wpk1, a.h1);
    wg_signal(cnt + (CNT_B + mt1) * CNT_STRIDE);
  }
  gemm2_pre(lds, n2, w, lane, a.wpk2);
  wg_wait(cnt + (CNT_B + pm) * CNT_STRIDE, 27);
  gemm2_phase<true>(lds, p64, n2, w, lane, a.h1, a.wpk2, a.x1, a.x2);
  wg_signal(cnt + (CNT_C + p64) * CNT_STRIDE);
  wg_wait(cnt + (CNT_C + p64) * CNT_STRIDE, 8);
  row_phase<1>(lds, t0, w, lane, a.x2, a.YT, a.Y, a.x3, a.xn2);
  wg_signal(cnt + (CNT_D + pm) * CNT_STRIDE);
  if (has1) {
    gemm1_pre(lds, nt1, w, lane, a.wpk1);
    wg_wait(cnt + (CNT_D + mt1) * CNT_STRIDE, 32);
    gemm1_phase(lds, mt1, nt1, w, lane, a.xn2, a.wpk1, a.h2);
    wg_signal(cnt + (CNT_E + mt1) * CNT_STRIDE);
  }
  gemm2_pre(lds, n2, w, lane, a.wpk2);
  wg_wait(cnt + (CNT_E + pm) * CNT_STRIDE, 27);
  gemm2_phase<false>(lds, p64, n2, w, lane, a.h2, a.wpk2, a.x3, a.out);
}

extern "C" void kernel_launch(void* const* d_in, const int* in_sizes, int n_in, void* d_out, int out_size,
                              void* d_ws, size_t ws_size, hipStream_t stream) {
  const u16* q = (const u16*)d_in[0];
  const u16* k = (const u16*)d_in[1];
  const u16* v = (const u16*)d_in[2];
  const u16* shift_k = (const u16*)d_in[3];
  const u16* ssm_k = (const u16*)d_in[4];
  const u16* ssm_D = (const u16*)d_in[5];
  const u16* wg = (const u16*)d_in[7];
  const u16* wu = (const u16*)d_in[8];
  const u16* wd = (const u16*)d_in[9];
  char* ws = (char*)d_ws;
  constexpr size_t SZ = (size_t)S * D * 2;
  u16* kvT = (u16*)(ws + 0 * SZ);
  u16* YT = (u16*)(ws + 1 * SZ);
  u16* wpk1 = (u16*)(ws + 10 * SZ);
  u16* wpk2 = (u16*)(ws + 12 * SZ);
  MainArgs a;
  a.q = q; a.YT = YT; a.Y = (u16*)(ws + 2 * SZ);
  a.x1 = (u16*)(ws + 3 * SZ); a.xn1 = (u16*)(ws + 4 * SZ); a.x2 = (u16*)(ws + 5 * SZ); a.x3 = (u16*)(ws + 6 * SZ);
  a.h1 = (u16*)(ws + 7 * SZ); a.xn2 = (u16*)(ws + 13 * SZ); a.h2 = (u16*)(ws + 14 * SZ);
  a.wpk1 = wpk1; a.wpk2 = wpk2; a.out = (u16*)d_out; a.cnt = (unsigned*)(ws + 17 * SZ);

  k_prep<<<320 + PK_WGS, 256, 0, stream>>>(k, v, shift_k, kvT, wg, wu, wd, wpk1, wpk2, a.cnt);
  k_conv<<<D, 256, 0, stream>>>(kvT, ssm_k, ssm_D, YT);
  k_main<<<256, 768, 0, stream>>>(a);
}
```

```cpp
#include <hip/hip_runtime.h>
#include <hip/hip_bf16.h>
#include <type_traits>

typedef unsigned short u16;
typedef __attribute__((ext_vector_type(4))) short s16x4;
typedef __attribute__((ext_vector_type(8))) short s16x8;
typedef __attribute__((ext_vector_type(16))) float f32x16;
typedef __attribute__((ext_vector_type(4))) float f32x4;
typedef __attribute__((ext_vector_type(4))) unsigned u32x4;
typedef __attribute__((ext_vector_type(2))) unsigned u32x2;
typedef __attribute__((address_space(3))) s16x4* lds_s16x4_ptr;

constexpr int S = 2048, D = 640, F = 1728;

__device__ __forceinline__ float bf2f(u16 b) { return __uint_as_float((unsigned)b << 16); }
__device__ __forceinline__ float bflo(unsigned p) { return __uint_as_float(p << 16); }
__device__ __forceinline__ float bfhi(unsigned p) { return __uint_as_float(p & 0xffff0000u); }
__device__ __forceinline__ u16 f2bf(float x) { __bf16 b = (__bf16)x; return __builtin_bit_cast(u16, b); }
__device__ __forceinline__ float rbf(float x) { return bf2f(f2bf(x)); }
__device__ __forceinline__ unsigned pack2(float lo, float hi) { return (unsigned)f2bf(lo) | ((unsigned)f2bf(hi) << 16); }

typedef __attribute__((ext_vector_type(2))) float f32x2;
typedef __attribute__((ext_vector_type(2))) __bf16 bf16x2_t;
__device__ __forceinline__ unsigned cvtpk(f32x2 v) { return __builtin_bit_cast(unsigned, __builtin_convertvector(v, bf16x2_t)); }
__device__ __forceinline__ f32x2 unpk(unsigned p) { f32x2 r = {bflo(p), bfhi(p)}; return r; }
__device__ __forceinline__ f32x2 rbf2(f32x2 v) { return unpk(cvtpk(v)); }

#define WAIT_V(n) asm volatile("s_waitcnt vmcnt(%0)" ::"n"(n) : "memory")
#define WAIT_L(n) asm volatile("s_waitcnt lgkmcnt(%0)" ::"n"(n) : "memory")

constexpr int PK2_CHUNKS = 8 * 27 * 64 * 10;
constexpr int PK_WGS = PK2_CHUNKS / 512;
__device__ __forceinline__ void pack_weights(int wgi, const u16* __restrict__ wd, u16* __restrict__ wpk2) {
  u32x4 v[2];
#pragma unroll
  for (int i = 0; i < 2; ++i) {
    const int c2 = wgi * 512 + i * 256 + threadIdx.x;
    const int rk = c2 / 10, ch = c2 - rk * 10;
    const int tk = rk >> 6, k = rk & 63;
    const int nt = tk / 27, kt = tk - nt * 27;
    v[i] = *(const u32x4*)(wd + (size_t)(kt * 64 + k) * D + nt * 80 + ch * 8);
  }
#pragma unroll
  for (int i = 0; i < 2; ++i) *(u32x4*)(wpk2 + (size_t)(wgi * 512 + i * 256 + threadIdx.x) * 8) = v[i];
}

__global__ __launch_bounds__(256) void k_prep(const u16* __restrict__ k, const u16* __restrict__ v,
                                              const u16* __restrict__ shift_k, u16* __restrict__ kvT,
                                              const u16* __restrict__ wd, u16* __restrict__ wpk2,
                                              unsigned* __restrict__ cnt) {
  __shared__ __attribute__((aligned(16))) unsigned tile[64][36];
  if (blockIdx.x == 0 && threadIdx.x < 64) cnt[threadIdx.x * 64] = 0u;
  if (blockIdx.x >= 320) { pack_weights(blockIdx.x - 320, wd, wpk2); return; }
  const int t0 = (blockIdx.x & 31) * 64, d0 = (blockIdx.x >> 5) * 64;
  const int tid = threadIdx.x;
  const int cd = (tid & 7) * 8, tp = tid >> 3;
  const int t = t0 + 2 * tp;
  u32x4 kk[5], vv[2], sw[4];
#pragma unroll
  for (int j = 0; j < 5; ++j) {
    u32x4 z = {0u, 0u, 0u, 0u};
    kk[j] = (t - 3 + j >= 0) ? *(const u32x4*)(k + (size_t)(t - 3 + j) * D + d0 + cd) : z;
  }
  vv[0] = *(const u32x4*)(v + (size_t)t * D + d0 + cd);
  vv[1] = *(const u32x4*)(v + (size_t)(t + 1) * D + d0 + cd);
#pragma unroll
  for (int i = 0; i < 4; ++i) sw[i] = *(const u32x4*)(shift_k + (size_t)(d0 + cd) * 4 + 8 * i);
#pragma unroll
  for (int cp = 0; cp < 4; ++cp) {
    f32x2 wj[4];
    wj[0] = (f32x2){bflo(sw[cp][0]), bflo(sw[cp][2])};
    wj[1] = (f32x2){bfhi(sw[cp][0]), bfhi(sw[cp][2])};
    wj[2] = (f32x2){bflo(sw[cp][1]), bflo(sw[cp][3])};
    wj[3] = (f32x2){bfhi(sw[cp][1]), bfhi(sw[cp][3])};
    f32x2 kf[5];
#pragma unroll
    for (int j = 0; j < 5; ++j) kf[j] = unpk(kk[j][cp]);
    unsigned o[2];
#pragma unroll
    for (int rr = 0; rr < 2; ++rr) {
      f32x2 ks = rbf2(kf[rr + 0] * wj[3]);
      ks = rbf2(ks + rbf2(kf[rr + 1] * wj[2]));
      ks = rbf2(ks + rbf2(kf[rr + 2] * wj[1]));
      ks = rbf2(ks + rbf2(kf[rr + 3] * wj[0]));
      o[rr] = cvtpk(ks * unpk(vv[rr][cp]));
    }
    tile[cd + 2 * cp][tp] = (o[0] & 0xffffu) | (o[1] << 16);
    tile[cd + 2 * cp + 1][tp] = (o[0] >> 16) | (o[1] & 0xffff0000u);
  }
  __syncthreads();
  {
    const int dl = tid >> 2, c = (tid & 3) * 8;
    u32x4 a = *(const u32x4*)&tile[dl][c];
    u32x4 b = *(const u32x4*)&tile[dl][c + 4];
    u16* dst = kvT + (size_t)(d0 + dl) * S + t0 + 2 * c;
    *(u32x4*)dst = a;
    *(u32x4*)(dst + 8) = b;
  }
}

constexpr int CV_T_BYTES = 2112 * 8;
constexpr int CV_U_OFF = 32768;
constexpr int CV_U_ROW = 144;
constexpr int CV_LDS = CV_U_OFF + 64 * CV_U_ROW;

__global__ __launch_bounds__(256) void k_conv(const u16* __restrict__ kvT, const u16* __restrict__ ssm_k,
                                              const u16* __restrict__ ssm_D, u16* __restrict__ YT) {
  __shared__ __attribute__((aligned(16))) char lds[CV_LDS];
  const int d = blockIdx.x;
  const int tid = threadIdx.x, lane = tid & 63, w = tid >> 6;
  const u16* h = ssm_k + (size_t)d * S;
  const u16* u = kvT + (size_t)d * S;
  {
    const int base = 2040 - 8 * tid;
    u32x4 hi = *(const u32x4*)(h + base);
    u32x4 z = {0u, 0u, 0u, 0u};
    u32x4 lo = (base >= 8) ? *(const u32x4*)(h + base - 8) : z;
    unsigned Dw[8] = {lo[0], lo[1], lo[2], lo[3], hi[0], hi[1], hi[2], hi[3]};
    unsigned ent[16];
#pragma unroll
    for (int c = 0; c < 4; ++c) {
      ent[4 * c + 0] = __builtin_amdgcn_alignbit(Dw[7 - c], Dw[7 - c], 16);
      ent[4 * c + 1] = __builtin_amdgcn_alignbit(Dw[6 - c], Dw[6 - c], 16);
      ent[4 * c + 2] = (Dw[7 - c] & 0xffffu) | (Dw[6 - c] & 0xffff0000u);
      ent[4 * c + 3] = (Dw[6 - c] & 0xffffu) | (Dw[5 - c] & 0xffff0000u);
    }
    u32x4* tp = (u32x4*)(lds + tid * 64);
#pragma unroll
    for (int c = 0; c < 4; ++c) {
      u32x4 q = {ent[4 * c], ent[4 * c + 1], ent[4 * c + 2], ent[4 * c + 3]};
      tp[c] = q;
    }
    if (tid < 32) *(u32x4*)(lds + 2048 * 8 + tid * 16) = z;
    u32x4 uu = *(const u32x4*)(u + tid * 8);
    *(u32x4*)(lds + CV_U_OFF + (32 + (tid >> 3)) * CV_U_ROW + (tid & 7) * 16) = uu;
    for (int i = tid; i < 32 * CV_U_ROW / 16; i += 256) *(u32x4*)(lds + CV_U_OFF + i * 16) = z;
  }
  __syncthreads();
  const int r = lane & 31, hh = lane >> 5;
  f32x16 acc0 = {}, acc1 = {};
  {
    const char* tb = lds + (2047 - r + 8 * hh) * 8;
    const char* ub = lds + CV_U_OFF + (r + 32) * CV_U_ROW + hh * 16;
    auto ldT = [&](int q) -> s16x8 {
      const char* p = tb - q * 128;
      s16x4 a = *(const s16x4*)p;
      s16x4 b = *(const s16x4*)(p + 32);
      return __builtin_shufflevector(a, b, 0, 1, 2, 3, 4, 5, 6, 7);
    };
    const int m0 = 8 * w;
    s16x8 Tm3 = ldT(4 * m0 - 3), Tm2 = ldT(4 * m0 - 2);
#pragma unroll 2
    for (int mm = 0; mm < 8; ++mm) {
      const int m = m0 + mm;
      s16x8 Tm1 = ldT(4 * m - 1), T0 = ldT(4 * m), T1 = ldT(4 * m + 1), T2 = ldT(4 * m + 2);
      const char* up = ub - m * CV_U_ROW;
      s16x8 U0 = *(const s16x8*)(up), U1 = *(const s16x8*)(up + 32), U2 = *(const s16x8*)(up + 64), U3 = *(const s16x8*)(up + 96);
      acc0 = __builtin_amdgcn_mfma_f32_32x32x16_bf16(U0, T0, acc0, 0, 0, 0);
      acc1 = __builtin_amdgcn_mfma_f32_32x32x16_bf16(U0, T2, acc1, 0, 0, 0);
      acc0 = __builtin_amdgcn_mfma_f32_32x32x16_bf16(U1, Tm1, acc0, 0, 0, 0);
      acc1 = __builtin_amdgcn_mfma_f32_32x32x16_bf16(U1, T1, acc1, 0, 0, 0);
      acc0 = __builtin_amdgcn_mfma_f32_32x32x16_bf16(U2, Tm2, acc0, 0, 0, 0);
      acc1 = __builtin_amdgcn_mfma_f32_32x32x16_bf16(U2, T0, acc1, 0, 0, 0);
      acc0 = __builtin_amdgcn_mfma_f32_32x32x16_bf16(U3, Tm3, acc0, 0, 0, 0);
      acc1 = __builtin_amdgcn_mfma_f32_32x32x16_bf16(U3, Tm1, acc1, 0, 0, 0);
      Tm3 = T1; Tm2 = T2;
    }
  }
  asm volatile("" :: "v"(acc0[0]), "v"(acc1[0]));
  __syncthreads();
  {
    float* P = (float*)lds + w * 2048;
#pragma unroll
    for (int reg = 0; reg < 16; ++reg) {
      const int i = (reg & 3) + 8 * (reg >> 2) + 4 * hh;
      P[64 * i + r] = acc0[reg];
      P[64 * i + 32 + r] = acc1[reg];
    }
  }
  __syncthreads();
  {
    const float* P = (const float*)lds + tid * 8;
    float s[8];
#pragma unroll
    for (int e = 0; e < 8; ++e) s[e] = 0.f;
#pragma unroll
    for (int ww = 0; ww < 4; ++ww) {
      f32x4 a = *(const f32x4*)(P + ww * 2048), b = *(const f32x4*)(P + ww * 2048 + 4);
#pragma unroll
      for (int e = 0; e < 4; ++e) { s[e] += a[e]; s[4 + e] += b[e]; }
    }
    u32x4 uu = *(const u32x4*)(lds + CV_U_OFF + (32 + (tid >> 3)) * CV_U_ROW + (tid & 7) * 16);
    const float Dv = bf2f(ssm_D[d]);
    u32x4 o;
#pragma unroll
    for (int e = 0; e < 4; ++e) {
      float y0 = rbf(s[2 * e]) + rbf(bflo(uu[e]) * Dv);
      float y1 = rbf(s[2 * e + 1]) + rbf(bfhi(uu[e]) * Dv);
      o[e] = pack2(y0, y1);
    }
    *(u32x4*)(YT + (size_t)d * S + tid * 8) = o;
  }
}

template <int CTRL> __device__ __forceinline__ float dpp_add(float v) {
  return v + __builtin_bit_cast(float, __builtin_amdgcn_update_dpp(0, __builtin_bit_cast(int, v), CTRL, 0xf, 0xf, true));
}
__device__ __forceinline__ float wave_sum(float v) {
  v = dpp_add<0xB1>(v);
  v = dpp_add<0x4E>(v);
  v = dpp_add<0x141>(v);
  v = dpp_add<0x140>(v);
  const int iv = __builtin_bit_cast(int, v);
  const float a = __builtin_bit_cast(float, __builtin_amdgcn_readlane(iv, 0));
  const float b = __builtin_bit_cast(float, __builtin_amdgcn_readlane(iv, 16));
  const float c = __builtin_bit_cast(float, __builtin_amdgcn_readlane(iv, 32));
  const float d = __builtin_bit_cast(float, __builtin_amdgcn_readlane(iv, 48));
  return (a + b) + (c + d);
}
__device__ __forceinline__ void ln_row(const f32x2 (&x)[5], unsigned (&o)[5]) {
  f32x2 s2 = (x[0] + x[1]) + (x[2] + x[3]) + x[4];
  const float mean = wave_sum(s2[0] + s2[1]) * (1.0f / D);
  f32x2 q2 = {0.f, 0.f};
#pragma unroll
  for (int i = 0; i < 5; ++i) { f32x2 dd = x[i] - mean; q2 += dd * dd; }
  const float var = rbf(wave_sum(q2[0] + q2[1]) * (1.0f / D));
  const float mb = rbf(mean);
  const float sd = rbf(__builtin_amdgcn_sqrtf(rbf(var + 1e-5f)));
  const float rs = __builtin_amdgcn_rcpf(sd);
#pragma unroll
  for (int i = 0; i < 5; ++i) o[i] = cvtpk(rbf2(x[i] - mb) * rs);
}

__device__ __forceinline__ unsigned lds_addr(const void* p) {
  return (unsigned)(uintptr_t)(const __attribute__((address_space(3))) char*)p;
}
template <bool SC1 = false>
__device__ __forceinline__ void glds16(const void* sbase, unsigned voff, unsigned ldst) {
  const unsigned long long pv = (unsigned long long)sbase;
  const unsigned long long ub = ((unsigned long long)(unsigned)__builtin_amdgcn_readfirstlane((unsigned)(pv >> 32)) << 32) |
                                (unsigned)__builtin_amdgcn_readfirstlane((unsigned)pv);
  const unsigned ul = __builtin_amdgcn_readfirstlane(ldst);
  unsigned keep;
  if (SC1)
    asm volatile("s_mov_b32 %0, m0\n\ts_mov_b32 m0, %3\n\ts_nop 0\n\tglobal_load_lds_dwordx4 %1, %2 sc1\n\ts_mov_b32 m0, %0"
                 : "=&s"(keep) : "v"(voff), "s"(ub), "s"(ul) : "memory");
  else
    asm volatile("s_mov_b32 %0, m0\n\ts_mov_b32 m0, %3\n\ts_nop 0\n\tglobal_load_lds_dwordx4 %1, %2\n\ts_mov_b32 m0, %0"
                 : "=&s"(keep) : "v"(voff), "s"(ub), "s"(ul) : "memory");
}
__device__ __forceinline__ s16x8 tr_frag(const char* p0, const char* p1) {
  s16x4 a = __builtin_amdgcn_ds_read_tr16_b64_v4i16((lds_s16x4_ptr)p0);
  s16x4 b = __builtin_amdgcn_ds_read_tr16_b64_v4i16((lds_s16x4_ptr)p1);
  return __builtin_shufflevector(a, b, 0, 1, 2, 3, 4, 5, 6, 7);
}

#define RLX_AGENT __ATOMIC_RELAXED, __HIP_MEMORY_SCOPE_AGENT
__device__ __forceinline__ void st16_sc1(void* p, u32x4 v) { asm volatile("global_store_dwordx4 %0, %1, off sc1\n\ts_nop 1" ::"v"(p), "v"(v) : "memory"); }
__device__ __forceinline__ void st8_sc1(void* p, u32x2 v) { asm volatile("global_store_dwordx2 %0, %1, off sc1\n\ts_nop 1" ::"v"(p), "v"(v) : "memory"); }
__device__ __forceinline__ void wg_signal(unsigned* cnt) {
  asm volatile("s_waitcnt vmcnt(0) lgkmcnt(0)" ::: "memory");
  __builtin_amdgcn_s_barrier();
  if (threadIdx.x == 0) __hip_atomic_fetch_add(cnt, 1u, RLX_AGENT);
}
__device__ __forceinline__ void wg_wait(unsigned* cnt, unsigned want) {
  if (threadIdx.x < 64) {
    unsigned spins = 0;
    while ((unsigned)__builtin_amdgcn_readfirstlane(__hip_atomic_load(cnt, RLX_AGENT)) < want) {
      __builtin_amdgcn_s_sleep(1);
      if (++spins > (1u << 22)) break;
    }
  }
  asm volatile("" ::: "memory");
  __builtin_amdgcn_s_barrier();
  asm volatile("" ::: "memory");
}

#define SCHED() __builtin_amdgcn_sched_barrier(0)
#define ILV_MFMA_DS()                                        \
  do {                                                       \
    __builtin_amdgcn_sched_group_barrier(0x008, 1, 0);       \
    __builtin_amdgcn_sched_group_barrier(0x100, 2, 0);       \
    __builtin_amdgcn_sched_group_barrier(0x008, 1, 0);       \
    __builtin_amdgcn_sched_group_barrier(0x100, 2, 0);       \
    __builtin_amdgcn_sched_group_barrier(0x008, 1, 0);       \
    __builtin_amdgcn_sched_group_barrier(0x100, 2, 0);       \
    __builtin_amdgcn_sched_group_barrier(0x008, 1, 0);       \
  } while (0)

__device__ __forceinline__ unsigned quad_get(unsigned v, int sel) {
  switch (sel) {
    case 1: return __builtin_amdgcn_update_dpp(0, (int)v, 0x55, 0xf, 0xf, true);
    case 2: return __builtin_amdgcn_update_dpp(0, (int)v, 0xAA, 0xf, 0xf, true);
    default: return __builtin_amdgcn_update_dpp(0, (int)v, 0xFF, 0xf, 0xf, true);
  }
}
template <int MODE>
__device__ __forceinline__ void row_phase(char* lds, int t0, int w, int lane, const u16* __restrict__ xin,
                                          const u16* __restrict__ YT, u16* __restrict__ Y,
                                          u16* __restrict__ xout, u16* __restrict__ xn) {
  u16 (*yt)[D + 8] = (u16 (*)[D + 8])lds;
  const int t = t0 + (w & 7);
  u32x4 xa = {0u, 0u, 0u, 0u}; unsigned xb = 0u;
  if (w < 8) {
    if (MODE == 0) {
      xa = *(const u32x4*)(xin + (size_t)t * D + 8 * lane);
      xb = *(const unsigned*)(xin + (size_t)t * D + 512 + 2 * lane);
    } else {
      const unsigned long long* xp = (const unsigned long long*)(xin + (size_t)t * D + 8 * lane);
      const unsigned long long v0 = __hip_atomic_load(xp, RLX_AGENT), v1 = __hip_atomic_load(xp + 1, RLX_AGENT);
      xa = (u32x4){(unsigned)v0, (unsigned)(v0 >> 32), (unsigned)v1, (unsigned)(v1 >> 32)};
      xb = __hip_atomic_load((const unsigned*)(xin + (size_t)t * D + 512 + 2 * lane), RLX_AGENT);
    }
  }
  u32x4 ya; unsigned yb;
  if (MODE == 0) {
    const int dd = w * 64 + lane;
    if (dd < D) {
      u32x4 q = *(const u32x4*)(YT + (size_t)dd * S + t0);
#pragma unroll
      for (int e = 0; e < 4; ++e) {
        yt[2 * e][dd] = (u16)(q[e] & 0xffffu);
        yt[2 * e + 1][dd] = (u16)(q[e] >> 16);
      }
    }
    __syncthreads();
  }
  if (w < 8) {
    if (MODE == 0) {
      ya = *(const u32x4*)&yt[w][8 * lane];
      yb = *(const unsigned*)&yt[w][512 + 2 * lane];
      *(u32x4*)(Y + (size_t)t * D + 8 * lane) = ya;
      *(unsigned*)(Y + (size_t)t * D + 512 + 2 * lane) = yb;
    } else {
      ya = *(const u32x4*)(Y + (size_t)t * D + 8 * lane);
      yb = *(const unsigned*)(Y + (size_t)t * D + 512 + 2 * lane);
    }
    f32x2 x[5] = {unpk(xa[0]), unpk(xa[1]), unpk(xa[2]), unpk(xa[3]), unpk(xb)};
    const unsigned yp[5] = {ya[0], ya[1], ya[2], ya[3], yb};
    unsigned n[5], xo[5];
    ln_row(x, n);
#pragma unroll
    for (int i = 0; i < 5; ++i) { xo[i] = cvtpk(x[i] + rbf2(unpk(n[i]) * unpk(yp[i]))); x[i] = unpk(xo[i]); }
    ln_row(x, n);
    const u32x4 xt = {xo[4], quad_get(xo[4], 1), quad_get(xo[4], 2), quad_get(xo[4], 3)};
    const u32x4 nt4 = {n[4], quad_get(n[4], 1), quad_get(n[4], 2), quad_get(n[4], 3)};
    const int sw = (t >> 1) & 7;
    u16* bp = xn + (size_t)(t >> 8) * (10 * 256 * 64) + (t & 255) * 64;
    st16_sc1(xout + (size_t)t * D + 8 * lane, (u32x4){xo[0], xo[1], xo[2], xo[3]});
    st16_sc1(bp + (lane >> 3) * (256 * 64) + (((lane & 7) ^ sw) * 8), (u32x4){n[0], n[1], n[2], n[3]});
    if ((lane & 3) == 0) {
      st16_sc1(xout + (size_t)t * D + 512 + 2 * lane, xt);
      st16_sc1(bp + (8 + (lane >> 5)) * (256 * 64) + ((((lane & 31) >> 2) ^ sw) * 8), nt4);
    }
  }
}

constexpr int G1_A = 256 * 64 * 2, G1_B = 64 * 128 * 2, G1_ST = G1_A + G1_B, G1_NS = 3;

__device__ __forceinline__ unsigned g1_boff(int nt, int lw, int i, int lane) {
  const int row = ((lw & 1) * 4 + i) * 8 + (lane >> 3);
  const int seg = ((lane >> 2) & 1) ^ ((row >> 1) & 1);
  return (unsigned)(row * F + nt * 64 + seg * 32 + (lane & 3) * 8) * 2;
}
__device__ __forceinline__ void gemm1_pre(char* lds, int nt, int w, int lane, const u16* __restrict__ wg, const u16* __restrict__ wu) {
  if (w >= 8) {
    const int lw = w - 8;
    const unsigned lbase = lds_addr(lds);
    const char* bmat = (const char*)((lw & 2) ? wu : wg);
#pragma unroll
    for (int st = 0; st < 3; ++st)
#pragma unroll
      for (int i = 0; i < 4; ++i) glds16(bmat + (size_t)st * 64 * F * 2, g1_boff(nt, lw, i, lane), lbase + st * G1_ST + G1_A + (lw * 4 + i) * 1024);
  }
}
__device__ __forceinline__ void gemm1_phase(char* lds, int mt, int nt, int w, int lane, const u16* __restrict__ xn,
                                            const u16* __restrict__ wg, const u16* __restrict__ wu, u16* __restrict__ hout) {
  constexpr int NT = D / 64;
  if (w >= 8) {
    const int lw = w - 8;
    const unsigned lbase = lds_addr(lds);
    const unsigned voff = lane * 16;
    const char* abase = (const char*)xn + (size_t)mt * 10 * 32768 + lw * 8192;
    const char* bmat = (const char*)((lw & 2) ? wu : wg);
    unsigned boffs[4];
#pragma unroll
    for (int i = 0; i < 4; ++i) boffs[i] = g1_boff(nt, lw, i, lane);
    auto stageA = [&](int buf, int kt) {
#pragma unroll
      for (int i = 0; i < 8; ++i) glds16<true>(abase + kt * 32768 + i * 1024, voff, lbase + buf * G1_ST + (lw * 8 + i) * 1024);
    };
    auto stageB = [&](int buf, int kt) {
#pragma unroll
      for (int i = 0; i < 4; ++i) glds16(bmat + (size_t)kt * 64 * F * 2, boffs[i], lbase + buf * G1_ST + G1_A + (lw * 4 + i) * 1024);
    };
    stageA(0, 0);
    stageA(1, 1);
    WAIT_V(8);
    __builtin_amdgcn_s_barrier();
    stageA(2, 2);
    WAIT_V(8);
    __builtin_amdgcn_s_barrier();
    stageB(0, 3); stageA(0, 3);
    int buf = 1;
#pragma unroll 1
    for (int kt = 1; kt + 3 < NT; ++kt) {
      WAIT_V(12);
      __builtin_amdgcn_s_barrier();
      stageB(buf, kt + 3); stageA(buf, kt + 3);
      buf = (buf == 2) ? 0 : buf + 1;
    }
    WAIT_V(12);
    __builtin_amdgcn_s_barrier();
    WAIT_V(0);
    __builtin_amdgcn_s_barrier();
  } else {
    const int wm = w >> 1, wn = w & 1;
    f32x16 ag[2] = {}, au[2] = {};
    const int r = lane & 31, hh = lane >> 5;
    const int arow = (wm * 64 + r) * 128;
    const int asw = (r >> 1) & 7;
    const int blk = (lane >> 4) & 1, q = (lane & 15) >> 2, p = lane & 3;
    const int boff = G1_A + (8 * hh + q) * 128 + ((wn ^ ((q >> 1) & 1)) * 64) + blk * 32 + p * 8;
    struct Frag { s16x8 a0, a1, bg, bu; };
    auto ldfrag = [&](const char* base, int ks) -> Frag {
      Frag f;
      const char* ap = base + arow + (((2 * ks + hh) ^ asw) * 16);
      const char* bp = base + boff + ks * 2048;
      f.bg = tr_frag(bp, bp + 512);
      f.a0 = *(const s16x8*)ap;
      f.bu = tr_frag(bp + 8192, bp + 8192 + 512);
      f.a1 = *(const s16x8*)(ap + 4096);
      return f;
    };
    auto mma = [&](const Frag& f) {
      ag[0] = __builtin_amdgcn_mfma_f32_32x32x16_bf16(f.bg, f.a0, ag[0], 0, 0, 0);
      au[0] = __builtin_amdgcn_mfma_f32_32x32x16_bf16(f.bu, f.a0, au[0], 0, 0, 0);
      ag[1] = __builtin_amdgcn_mfma_f32_32x32x16_bf16(f.bg, f.a1, ag[1], 0, 0, 0);
      au[1] = __builtin_amdgcn_mfma_f32_32x32x16_bf16(f.bu, f.a1, au[1], 0, 0, 0);
    };
    asm volatile("" ::: "memory");
    __builtin_amdgcn_s_barrier();
    asm volatile("" ::: "memory");
    Frag f0_ = ldfrag(lds, 0);
    int buf = 0;
#pragma unroll 1
    for (int kt = 0; kt < NT - 1; ++kt) {
      const char* base = lds + buf * G1_ST;
      SCHED();
      Frag f1_ = ldfrag(base, 1);
      mma(f0_);
      ILV_MFMA_DS();
      SCHED();
      Frag f2_ = ldfrag(base, 2);
      mma(f1_);
      ILV_MFMA_DS();
      SCHED();
      Frag f3_ = ldfrag(base, 3);
      mma(f2_);
      ILV_MFMA_DS();
      SCHED();
      buf = (buf == 2) ? 0 : buf + 1;
      WAIT_L(0);
      __builtin_amdgcn_s_barrier();
      asm volatile("" ::: "memory");
      SCHED();
      f0_ = ldfrag(lds + buf * G1_ST, 0);
      mma(f3_);
      ILV_MFMA_DS();
      SCHED();
    }
    {
      const char* base = lds + buf * G1_ST;
      Frag f1_ = ldfrag(base, 1);
      mma(f0_);
      ILV_MFMA_DS();
      SCHED();
      Frag f2_ = ldfrag(base, 2);
      mma(f1_);
      ILV_MFMA_DS();
      SCHED();
      Frag f3_ = ldfrag(base, 3);
      mma(f2_);
      ILV_MFMA_DS();
      SCHED();
      mma(f3_);
    }
    constexpr int HROW = 80;
    char* slab = lds + G1_ST + w * (64 * HROW);
    u16* hbase = hout + ((size_t)(mt * 4 + wm) * 27 + nt) * 4096;
#pragma unroll
    for (int mi = 0; mi < 2; ++mi) {
      char* hp = slab + (mi * 32 + r) * HROW + 8 * hh;
#pragma unroll
      for (int gq = 0; gq < 4; ++gq) {
        u32x2 o;
#pragma unroll
        for (int e = 0; e < 2; ++e) {
          const f32x2 gv = rbf2((f32x2){ag[mi][4 * gq + 2 * e], ag[mi][4 * gq + 2 * e + 1]}) * -1.44269504f;
          const f32x2 uv = rbf2((f32x2){au[mi][4 * gq + 2 * e], au[mi][4 * gq + 2 * e + 1]});
          const f32x2 den = (f32x2){__builtin_amdgcn_exp2f(gv[0]), __builtin_amdgcn_exp2f(gv[1])} + 1.0f;
          const f32x2 sg = rbf2((f32x2){__builtin_amdgcn_rcpf(den[0]), __builtin_amdgcn_rcpf(den[1])});
          o[e] = cvtpk(sg * uv);
        }
        *(u32x2*)(hp + 16 * gq) = o;
      }
      asm volatile("s_waitcnt lgkmcnt(0)" ::: "memory");
#pragma unroll
      for (int i = 0; i < 2; ++i) {
        const int row = mi * 32 + i * 16 + (lane >> 2), c = lane & 3;
        u32x4 o = *(const u32x4*)(slab + row * HROW + c * 16);
        st16_sc1(hbase + row * 64 + (((wn * 4 + c) ^ ((row >> 1) & 7)) * 8), o);
      }
    }
  }
}

constexpr int G2_A = 64 * 64 * 2, G2_B = 64 * 80 * 2, G2_ST = G2_A + G2_B, G2_NS = 6, G2_LAG = 1;

__device__ __forceinline__ void gemm2_pre(char* lds, int nt, int w, int lane, const u16* __restrict__ wpk) {
  if (w >= 4) {
    const int lw = w - 4;
    const unsigned lbase = lds_addr(lds);
    const char* bbase = (const char*)wpk + (size_t)nt * 27 * 10240 + lw * 1024;
#pragma unroll
    for (int st = 0; st < G2_NS; ++st) {
      glds16(bbase + st * 10240, lane * 16, lbase + st * G2_ST + G2_A + lw * 1024);
      if (lw < 2) glds16(bbase + st * 10240 + 8192, lane * 16, lbase + st * G2_ST + G2_A + (lw + 8) * 1024);
    }
  }
}
template <bool HANDOFF>
__device__ __forceinline__ void gemm2_phase(char* lds, int mt, int nt, int w, int lane, const u16* __restrict__ hin,
                                            const u16* __restrict__ wpk, const u16* __restrict__ xres,
                                            u16* __restrict__ out) {
  constexpr int NT = F / 64;
  const int m0 = mt * 64, n0 = nt * 80;
  if (w >= 4) {
    const int lw = w - 4;
    const unsigned lbase = lds_addr(lds);
    const unsigned voff = lane * 16;
    const char* abase = (const char*)hin + (size_t)mt * 27 * 8192 + lw * 1024;
    const char* bbase = (const char*)wpk + (size_t)nt * 27 * 10240 + lw * 1024;
    auto stageA = [&](int buf, int kt) { glds16<true>(abase + kt * 8192, voff, lbase + buf * G2_ST + lw * 1024); };
    auto stage = [&](int buf, int kt) {
      const unsigned a = lbase + buf * G2_ST;
      glds16<true>(abase + kt * 8192, voff, a + lw * 1024);
      glds16(bbase + kt * 10240, voff, a + G2_A + lw * 1024);
      if (lw < 2) glds16(bbase + kt * 10240 + 8192, voff, a + G2_A + (lw + 8) * 1024);
    };
    stageA(0, 0);
    stageA(1, 1);
    WAIT_V(1);
    __builtin_amdgcn_s_barrier();
#pragma unroll
    for (int i = 2; i < G2_NS; ++i) stageA(i, i);
    auto loader_loop = [&](auto per_tile_c) {
      constexpr int PT = decltype(per_tile_c)::value;
      WAIT_V(4); __builtin_amdgcn_s_barrier();
      WAIT_V(3); __builtin_amdgcn_s_barrier(); stage(0, 6);
      WAIT_V(2 + PT); __builtin_amdgcn_s_barrier(); stage(1, 7);
      WAIT_V(1 + 2 * PT); __builtin_amdgcn_s_barrier(); stage(2, 8);
      int buf = 3;
#pragma unroll 1
      for (int kt = 4; kt - 1 + G2_NS < NT; ++kt) {
        WAIT_V(PT * 3);
        __builtin_amdgcn_s_barrier();
        stage(buf, kt - 1 + G2_NS);
        buf = (buf == G2_NS - 1) ? 0 : buf + 1;
      }
#pragma unroll
      for (int i = G2_NS - 2; i >= 1; --i) { WAIT_V(PT * (i - 1)); __builtin_amdgcn_s_barrier(); }
    };
    if (lw < 2) loader_loop(std::integral_constant<int, 3>{}); else loader_loop(std::integral_constant<int, 2>{});
    __builtin_amdgcn_s_barrier();
    __builtin_amdgcn_s_barrier();
  } else {
    const int wm = w & 1, kh = w >> 1;
    f32x4 acc[2][5] = {};
    const int r = lane & 15, g4 = lane >> 4;
    const int aoff0 = (wm * 32 + r) * 128 + (((4 * kh + g4) ^ ((r >> 1) & 7)) * 16);
    const int q = (lane & 15) >> 2, p = lane & 3;
    const int boff = G2_A + (32 * kh + 8 * g4 + q) * 160 + p * 8;
    struct Frag { s16x8 a[2], b[5]; };
    auto ldfrag = [&](const char* base) -> Frag {
      Frag f;
      f.a[0] = *(const s16x8*)(base + aoff0);
      f.a[1] = *(const s16x8*)(base + aoff0 + 2048);
#pragma unroll
      for (int c = 0; c < 5; ++c) f.b[c] = tr_frag(base + boff + c * 32, base + boff + c * 32 + 640);
      return f;
    };
    auto mma = [&](const Frag& f) {
#pragma unroll
      for (int c = 0; c < 5; ++c) {
        acc[0][c] = __builtin_amdgcn_mfma_f32_16x16x32_bf16(f.b[c], f.a[0], acc[0][c], 0, 0, 0);
        acc[1][c] = __builtin_amdgcn_mfma_f32_16x16x32_bf16(f.b[c], f.a[1], acc[1][c], 0, 0, 0);
      }
    };
    u32x2 xr[2][5];
    if (kh == 0) {
#pragma unroll
      for (int mi = 0; mi < 2; ++mi)
#pragma unroll
        for (int c = 0; c < 5; ++c) {
          const unsigned long long v = __hip_atomic_load((const unsigned long long*)(xres + (size_t)(m0 + wm * 32 + mi * 16 + r) * D + n0 + c * 16 + 4 * g4), RLX_AGENT);
          xr[mi][c] = (u32x2){(unsigned)v, (unsigned)(v >> 32)};
        }
    }
    asm volatile("" ::: "memory");
    __builtin_amdgcn_s_barrier();
    asm volatile("" ::: "memory");
    Frag fa = ldfrag(lds);
    int buf = 0;
#pragma unroll 1
    for (int kt = 0; kt < NT - 1; kt += 2) {
      buf = (buf == G2_NS - 1) ? 0 : buf + 1;
      __builtin_amdgcn_s_barrier();
      asm volatile("" ::: "memory");
      SCHED();
      Frag fb = ldfrag(lds + buf * G2_ST);
      mma(fa);
#pragma unroll
      for (int i = 0; i < 6; ++i) { __builtin_amdgcn_sched_group_barrier(0x008, 1, 0); __builtin_amdgcn_sched_group_barrier(0x100, 2, 0); }
      __builtin_amdgcn_sched_group_barrier(0x008, 4, 0);
      SCHED();
      buf = (buf == G2_NS - 1) ? 0 : buf + 1;
      __builtin_amdgcn_s_barrier();
      asm volatile("" ::: "memory");
      SCHED();
      fa = ldfrag(lds + buf * G2_ST);
      mma(fb);
#pragma unroll
      for (int i = 0; i < 6; ++i) { __builtin_amdgcn_sched_group_barrier(0x008, 1, 0); __builtin_amdgcn_sched_group_barrier(0x100, 2, 0); }
      __builtin_amdgcn_sched_group_barrier(0x008, 4, 0);
      SCHED();
    }
    mma(fa);
    WAIT_L(0);
    __builtin_amdgcn_s_barrier();
    asm volatile("" ::: "memory");
    constexpr int PROW = 336;
    if (kh == 1) {
#pragma unroll
      for (int mi = 0; mi < 2; ++mi)
#pragma unroll
        for (int c = 0; c < 5; ++c)
          *(f32x4*)(lds + (wm * 32 + mi * 16 + r) * PROW + (c * 16 + 4 * g4) * 4) = acc[mi][c];
    }
    WAIT_L(0);
    __builtin_amdgcn_s_barrier();
    asm volatile("" ::: "memory");
    if (kh == 0) {
#pragma unroll
      for (int mi = 0; mi < 2; ++mi) {
        const int row = m0 + wm * 32 + mi * 16 + r;
#pragma unroll
        for (int c = 0; c < 5; ++c) {
          f32x4 o = *(const f32x4*)(lds + (wm * 32 + mi * 16 + r) * PROW + (c * 16 + 4 * g4) * 4);
          const size_t idx = (size_t)row * D + n0 + c * 16 + 4 * g4;
          const u32x2 xv = xr[mi][c];
          u32x2 ov = {cvtpk(unpk(xv[0]) + rbf2((f32x2){acc[mi][c][0] + o[0], acc[mi][c][1] + o[1]})),
                      cvtpk(unpk(xv[1]) + rbf2((f32x2){acc[mi][c][2] + o[2], acc[mi][c][3] + o[3]}))};
          if (HANDOFF) st8_sc1(out + idx, ov); else *(u32x2*)(out + idx) = ov;
        }
      }
    }
  }
}

constexpr int CNT_STRIDE = 64;
constexpr int CNT_A = 0, CNT_B = 8, CNT_C = 16, CNT_D = 48, CNT_E = 56, CNT_N = 64;

struct MainArgs {
  const u16* q; const u16* YT; u16* Y; u16* x1; u16* x2; u16* x3; u16* xn1; u16* xn2; u16* h1; u16* h2;
  const u16* wg; const u16* wu; const u16* wpk2; u16* out; unsigned* cnt;
};

__global__ __launch_bounds__(768) void k_main(MainArgs a) {
  __shared__ __attribute__((aligned(1024))) char lds[G1_NS * G1_ST];
  const int id = blockIdx.x;
  const int g = id & 7, j = id >> 3;
  const int tid = threadIdx.x, lane = tid & 63;
  const int w = __builtin_amdgcn_readfirstlane(tid >> 6);
  const int p64 = g * 4 + (j & 3), n2 = j >> 2;
  const int t0 = p64 * 64 + n2 * 8;
  const int pm = p64 >> 2;
  const int mt1 = (g & 1) * 4 + (j & 3), nj = j >> 2, nt1 = (g >> 1) * 7 + nj;
  const bool has1 = (nj < 7) && (nt1 < 27);
  unsigned* cnt = a.cnt;

  row_phase<0>(lds, t0, w, lane, a.q, a.YT, a.Y, a.x1, a.xn1);
  wg_signal(cnt + (CNT_A + pm) * CNT_STRIDE);
  if (has1) {
    gemm1_pre(lds, nt1, w, lane, a.wg, a.wu);
    wg_wait(cnt + (CNT_A + mt1) * CNT_STRIDE, 32);
    gemm1_phase(lds, mt1, nt1, w, lane, a.xn1, a.wg, a.wu, a.h1);
    wg_signal(cnt + (CNT_B + mt1) * CNT_STRIDE);
  }
  gemm2_pre(lds, n2, w, lane, a.wpk2);
  wg_wait(cnt + (CNT_B + pm) * CNT_STRIDE, 27);
  gemm2_phase<true>(lds, p64, n2, w, lane, a.h1, a.wpk2, a.x1, a.x2);
  wg_signal(cnt + (CNT_C + p64) * CNT_STRIDE);
  wg_wait(cnt + (CNT_C + p64) * CNT_STRIDE, 8);
  row_phase<1>(lds, t0, w, lane, a.x2, a.YT, a.Y, a.x3, a.xn2);
  wg_signal(cnt + (CNT_D + pm) * CNT_STRIDE);
  if (has1) {
    gemm1_pre(lds, nt1, w, lane, a.wg, a.wu);
    wg_wait(cnt + (CNT_D + mt1) * CNT_STRIDE, 32);
    gemm1_phase(lds, mt1, nt1, w, lane, a.xn2, a.wg, a.wu, a.h2);
    wg_signal(cnt + (CNT_E + mt1) * CNT_STRIDE);
  }
  gemm2_pre(lds, n2, w, lane, a.wpk2);
  wg_wait(cnt + (CNT_E + pm) * CNT_STRIDE, 27);
  gemm2_phase<false>(lds, p64, n2, w, lane, a.h2, a.wpk2, a.x3, a.out);
}

extern "C" void kernel_launch(void* const* d_in, const int* in_sizes, int n_in, void* d_out, int out_size,
                              void* d_ws, size_t ws_size, hipStream_t stream) {
  const u16* q = (const u16*)d_in[0];
  const u16* k = (const u16*)d_in[1];
  const u16* v = (const u16*)d_in[2];
  const u16* shift_k = (const u16*)d_in[3];
  const u16* ssm_k = (const u16*)d_in[4];
  const u16* ssm_D = (const u16*)d_in[5];
  const u16* wg = (const u16*)d_in[7];
  const u16* wu = (const u16*)d_in[8];
  const u16* wd = (const u16*)d_in[9];
  char* ws = (char*)d_ws;
  constexpr size_t SZ = (size_t)S * D * 2;
  u16* kvT = (u16*)(ws + 0 * SZ);
  u16* YT = (u16*)(ws + 1 * SZ);
  u16* wpk2 = (u16*)(ws + 12 * SZ);
  MainArgs a;
  a.q = q; a.YT = YT; a.Y = (u16*)(ws + 2 * SZ);
  a.x1 = (u16*)(ws + 3 * SZ); a.xn1 = (u16*)(ws + 4 * SZ); a.x2 = (u16*)(ws + 5 * SZ); a.x3 = (u16*)(ws + 6 * SZ);
  a.h1 = (u16*)(ws + 7 * SZ); a.xn2 = (u16*)(ws + 13 * SZ); a.h2 = (u16*)(ws + 14 * SZ);
  a.wg = wg; a.wu = wu; a.wpk2 = wpk2; a.out = (u16*)d_out; a.cnt = (unsigned*)(ws + 17 * SZ);

  k_prep<<<320 + PK_WGS, 256, 0, stream>>>(k, v, shift_k, kvT, wd, wpk2, a.cnt);
  k_conv<<<D, 256, 0, stream>>>(kvT, ssm_k, ssm_D, YT);
  k_main<<<256, 768, 0, stream>>>(a);
}
```

```cpp
#include <hip/hip_runtime.h>
#include <hip/hip_bf16.h>
#include <type_traits>

typedef unsigned short u16;
typedef __attribute__((ext_vector_type(4))) short s16x4;
typedef __attribute__((ext_vector_type(8))) short s16x8;
typedef __attribute__((ext_vector_type(16))) float f32x16;
typedef __attribute__((ext_vector_type(4))) float f32x4;
typedef __attribute__((ext_vector_type(4))) unsigned u32x4;
typedef __attribute__((ext_vector_type(2))) unsigned u32x2;
typedef __attribute__((address_space(3))) s16x4* lds_s16x4_ptr;

constexpr int S = 2048, D = 640, F = 1728;

__device__ __forceinline__ float bf2f(u16 b) { return __uint_as_float((unsigned)b << 16); }
__device__ __forceinline__ float bflo(unsigned p) { return __uint_as_float(p << 16); }
__device__ __forceinline__ float bfhi(unsigned p) { return __uint_as_float(p & 0xffff0000u); }
__device__ __forceinline__ u16 f2bf(float x) { __bf16 b = (__bf16)x; return __builtin_bit_cast(u16, b); }
__device__ __forceinline__ float rbf(float x) { return bf2f(f2bf(x)); }
__device__ __forceinline__ unsigned pack2(float lo, float hi) { return (unsigned)f2bf(lo) | ((unsigned)f2bf(hi) << 16); }

typedef __attribute__((ext_vector_type(2))) float f32x2;
typedef __attribute__((ext_vector_type(2))) __bf16 bf16x2_t;
__device__ __forceinline__ unsigned cvtpk(f32x2 v) { return __builtin_bit_cast(unsigned, __builtin_convertvector(v, bf16x2_t)); }
__device__ __forceinline__ f32x2 unpk(unsigned p) { f32x2 r = {bflo(p), bfhi(p)}; return r; }
__device__ __forceinline__ f32x2 rbf2(f32x2 v) { return unpk(cvtpk(v)); }

#define WAIT_V(n) asm volatile("s_waitcnt vmcnt(%0)" ::"n"(n) : "memory")
#define WAIT_L(n) asm volatile("s_waitcnt lgkmcnt(%0)" ::"n"(n) : "memory")

constexpr int PK2_CHUNKS = 8 * 27 * 64 * 10;
constexpr int PK_WGS = PK2_CHUNKS / 512;
__device__ __forceinline__ void pack_weights(int wgi, const u16* __restrict__ wd, u16* __restrict__ wpk2) {
  u32x4 v[2];
#pragma unroll
  for (int i = 0; i < 2; ++i) {
    const int c2 = wgi * 512 + i * 256 + threadIdx.x;
    const int rk = c2 / 10, ch = c2 - rk * 10;
    const int tk = rk >> 6, k = rk & 63;
    const int nt = tk / 27, kt = tk - nt * 27;
    v[i] = *(const u32x4*)(wd + (size_t)(kt * 64 + k) * D + nt * 80 + ch * 8);
  }
#pragma unroll
  for (int i = 0; i < 2; ++i) *(u32x4*)(wpk2 + (size_t)(wgi * 512 + i * 256 + threadIdx.x) * 8) = v[i];
}

__global__ __launch_bounds__(256) void k_prep(const u16* __restrict__ k, const u16* __restrict__ v,
                                              const u16* __restrict__ shift_k, u16* __restrict__ kvT,
                                              const u16* __restrict__ wd, u16* __restrict__ wpk2,
                                              unsigned* __restrict__ cnt) {
  __shared__ __attribute__((aligned(16))) unsigned tile[64][36];
  if (blockIdx.x == 0 && threadIdx.x < 64) cnt[threadIdx.x * 64] = 0u;
  if (blockIdx.x >= 320) { pack_weights(blockIdx.x - 320, wd, wpk2); return; }
  const int t0 = (blockIdx.x & 31) * 64, d0 = (blockIdx.x >> 5) * 64;
  const int tid = threadIdx.x;
  const int cd = (tid & 7) * 8, tp = tid >> 3;
  const int t = t0 + 2 * tp;
  u32x4 kk[5], vv[2], sw[4];
#pragma unroll
  for (int j = 0; j < 5; ++j) {
    u32x4 z = {0u, 0u, 0u, 0u};
    kk[j] = (t - 3 + j >= 0) ? *(const u32x4*)(k + (size_t)(t - 3 + j) * D + d0 + cd) : z;
  }
  vv[0] = *(const u32x4*)(v + (size_t)t * D + d0 + cd);
  vv[1] = *(const u32x4*)(v + (size_t)(t + 1) * D + d0 + cd);
#pragma unroll
  for (int i = 0; i < 4; ++i) sw[i] = *(const u32x4*)(shift_k + (size_t)(d0 + cd) * 4 + 8 * i);
#pragma unroll
  for (int cp = 0; cp < 4; ++cp) {
    f32x2 wj[4];
    wj[0] = (f32x2){bflo(sw[cp][0]), bflo(sw[cp][2])};
    wj[1] = (f32x2){bfhi(sw[cp][0]), bfhi(sw[cp][2])};
    wj[2] = (f32x2){bflo(sw[cp][1]), bflo(sw[cp][3])};
    wj[3] = (f32x2){bfhi(sw[cp][1]), bfhi(sw[cp][3])};
    f32x2 kf[5];
#pragma unroll
    for (int j = 0; j < 5; ++j) kf[j] = unpk(kk[j][cp]);
    unsigned o[2];
#pragma unroll
    for (int rr = 0; rr < 2; ++rr) {
      f32x2 ks = rbf2(kf[rr + 0] * wj[3]);
      ks = rbf2(ks + rbf2(kf[rr + 1] * wj[2]));
      ks = rbf2(ks + rbf2(kf[rr + 2] * wj[1]));
      ks = rbf2(ks + rbf2(kf[rr + 3] * wj[0]));
      o[rr] = cvtpk(ks * unpk(vv[rr][cp]));
    }
    tile[cd + 2 * cp][tp] = (o[0] & 0xffffu) | (o[1] << 16);
    tile[cd + 2 * cp + 1][tp] = (o[0] >> 16) | (o[1] & 0xffff0000u);
  }
  __syncthreads();
  {
    const int dl = tid >> 2, c = (tid & 3) * 8;
    u32x4 a = *(const u32x4*)&tile[dl][c];
    u32x4 b = *(const u32x4*)&tile[dl][c + 4];
    u16* dst = kvT + (size_t)(d0 + dl) * S + t0 + 2 * c;
    *(u32x4*)dst = a;
    *(u32x4*)(dst + 8) = b;
  }
}

constexpr int CV_U_OFF = 32768;
constexpr int CV_U_ROW = 144;
constexpr int CV_LDS = CV_U_OFF + 64 * CV_U_ROW;

__global__ __launch_bounds__(256) void k_conv(const u16* __restrict__ kvT, const u16* __restrict__ ssm_k,
                                              const u16* __restrict__ ssm_D, u16* __restrict__ YT) {
  __shared__ __attribute__((aligned(16))) char lds[CV_LDS];
  const int d = blockIdx.x;
  const int tid = threadIdx.x, lane = tid & 63, w = tid >> 6;
  const u16* h = ssm_k + (size_t)d * S;
  const u16* u = kvT + (size_t)d * S;
  const u32x4 z = {0u, 0u, 0u, 0u};
  const int base = 2040 - 8 * tid;
  const u32x4 hi = *(const u32x4*)(h + base);
  const u32x4 lo = (base >= 8) ? *(const u32x4*)(h + base - 8) : z;
  const u32x4 uu = *(const u32x4*)(u + tid * 8);
  const float Dv = bf2f(ssm_D[d]);
  if (tid < 32) *(u32x4*)(lds + 2048 * 8 + tid * 16) = z;
  for (int i = tid; i < 32 * CV_U_ROW / 16; i += 256) *(u32x4*)(lds + CV_U_OFF + i * 16) = z;
  {
    const unsigned Dw[8] = {lo[0], lo[1], lo[2], lo[3], hi[0], hi[1], hi[2], hi[3]};
    u32x4* tp = (u32x4*)(lds + tid * 64);
#pragma unroll
    for (int c = 0; c < 4; ++c) {
      u32x4 q;
      q[0] = __builtin_amdgcn_alignbit(Dw[7 - c], Dw[7 - c], 16);
      q[1] = __builtin_amdgcn_alignbit(Dw[6 - c], Dw[6 - c], 16);
      q[2] = (Dw[7 - c] & 0xffffu) | (Dw[6 - c] & 0xffff0000u);
      q[3] = (Dw[6 - c] & 0xffffu) | (Dw[5 - c] & 0xffff0000u);
      tp[c] = q;
    }
    *(u32x4*)(lds + CV_U_OFF + (32 + (tid >> 3)) * CV_U_ROW + (tid & 7) * 16) = uu;
  }
  __syncthreads();
  const int r = lane & 31, hh = lane >> 5;
  f32x16 acc0 = {}, acc1 = {};
  {
    const char* tb = lds + (2047 - r + 8 * hh) * 8;
    const char* ub = lds + CV_U_OFF + (r + 32) * CV_U_ROW + hh * 16;
    auto ldT = [&](int q) -> s16x8 {
      const char* p = tb - q * 128;
      s16x4 a = *(const s16x4*)p;
      s16x4 b = *(const s16x4*)(p + 32);
      return __builtin_shufflevector(a, b, 0, 1, 2, 3, 4, 5, 6, 7);
    };
    const int m0 = 8 * w;
    s16x8 Tm3 = ldT(4 * m0 - 3), Tm2 = ldT(4 * m0 - 2);
#pragma unroll 2
    for (int mm = 0; mm < 8; ++mm) {
      const int m = m0 + mm;
      s16x8 Tm1 = ldT(4 * m - 1), T0 = ldT(4 * m), T1 = ldT(4 * m + 1), T2 = ldT(4 * m + 2);
      const char* up = ub - m * CV_U_ROW;
      s16x8 U0 = *(const s16x8*)(up), U1 = *(const s16x8*)(up + 32), U2 = *(const s16x8*)(up + 64), U3 = *(const s16x8*)(up + 96);
      acc0 = __builtin_amdgcn_mfma_f32_32x32x16_bf16(U0, T0, acc0, 0, 0, 0);
      acc1 = __builtin_amdgcn_mfma_f32_32x32x16_bf16(U0, T2, acc1, 0, 0, 0);
      acc0 = __builtin_amdgcn_mfma_f32_32x32x16_bf16(U1, Tm1, acc0, 0, 0, 0);
      acc1 = __builtin_amdgcn_mfma_f32_32x32x16_bf16(U1, T1, acc1, 0, 0, 0);
      acc0 = __builtin_amdgcn_mfma_f32_32x32x16_bf16(U2, Tm2, acc0, 0, 0, 0);
      acc1 = __builtin_amdgcn_mfma_f32_32x32x16_bf16(U2, T0, acc1, 0, 0, 0);
      acc0 = __builtin_amdgcn_mfma_f32_32x32x16_bf16(U3, Tm3, acc0, 0, 0, 0);
      acc1 = __builtin_amdgcn_mfma_f32_32x32x16_bf16(U3, Tm1, acc1, 0, 0, 0);
      Tm3 = T1; Tm2 = T2;
    }
  }
  __syncthreads();
  {
    float* P = (float*)lds + w * 2048;
#pragma unroll
    for (int reg = 0; reg < 16; ++reg) {
      const int i = (reg & 3) + 8 * (reg >> 2) + 4 * hh;
      P[64 * i + r] = acc0[reg];
      P[64 * i + 32 + r] = acc1[reg];
    }
  }
  __syncthreads();
  {
    const float* P = (const float*)lds + tid * 8;
    f32x4 sa = *(const f32x4*)P, sb = *(const f32x4*)(P + 4);
#pragma unroll
    for (int ww = 1; ww < 4; ++ww) { sa += *(const f32x4*)(P + ww * 2048); sb += *(const f32x4*)(P + ww * 2048 + 4); }
    u32x4 o;
    o[0] = cvtpk(rbf2((f32x2){sa[0], sa[1]}) + rbf2(unpk(uu[0]) * Dv));
    o[1] = cvtpk(rbf2((f32x2){sa[2], sa[3]}) + rbf2(unpk(uu[1]) * Dv));
    o[2] = cvtpk(rbf2((f32x2){sb[0], sb[1]}) + rbf2(unpk(uu[2]) * Dv));
    o[3] = cvtpk(rbf2((f32x2){sb[2], sb[3]}) + rbf2(unpk(uu[3]) * Dv));
    *(u32x4*)(YT + (size_t)d * S + tid * 8) = o;
  }
}

template <int CTRL> __device__ __forceinline__ float dpp_add(float v) {
  return v + __builtin_bit_cast(float, __builtin_amdgcn_update_dpp(0, __builtin_bit_cast(int, v), CTRL, 0xf, 0xf, true));
}
__device__ __forceinline__ float wave_sum(float v) {
  v = dpp_add<0xB1>(v);
  v = dpp_add<0x4E>(v);
  v = dpp_add<0x141>(v);
  v = dpp_add<0x140>(v);
  const int iv = __builtin_bit_cast(int, v);
  const float a = __builtin_bit_cast(float, __builtin_amdgcn_readlane(iv, 0));
  const float b = __builtin_bit_cast(float, __builtin_amdgcn_readlane(iv, 16));
  const float c = __builtin_bit_cast(float, __builtin_amdgcn_readlane(iv, 32));
  const float d = __builtin_bit_cast(float, __builtin_amdgcn_readlane(iv, 48));
  return (a + b) + (c + d);
}
__device__ __forceinline__ void ln_row(const f32x2 (&x)[5], unsigned (&o)[5]) {
  f32x2 s2 = (x[0] + x[1]) + (x[2] + x[3]) + x[4];
  const float mean = wave_sum(s2[0] + s2[1]) * (1.0f / D);
  f32x2 q2 = {0.f, 0.f};
#pragma unroll
  for (int i = 0; i < 5; ++i) { f32x2 dd = x[i] - mean; q2 += dd * dd; }
  const float var = rbf(wave_sum(q2[0] + q2[1]) * (1.0f / D));
  const float mb = rbf(mean);
  const float sd = rbf(__builtin_amdgcn_sqrtf(rbf(var + 1e-5f)));
  const float rs = __builtin_amdgcn_rcpf(sd);
#pragma unroll
  for (int i = 0; i < 5; ++i) o[i] = cvtpk(rbf2(x[i] - mb) * rs);
}

__device__ __forceinline__ unsigned lds_addr(const void* p) {
  return (unsigned)(uintptr_t)(const __attribute__((address_space(3))) char*)p;
}
template <bool SC1 = false>
__device__ __forceinline__ void glds16(const void* sbase, unsigned voff, unsigned ldst) {
  const unsigned long long pv = (unsigned long long)sbase;
  const unsigned long long ub = ((unsigned long long)(unsigned)__builtin_amdgcn_readfirstlane((unsigned)(pv >> 32)) << 32) |
                                (unsigned)__builtin_amdgcn_readfirstlane((unsigned)pv);
  const unsigned ul = __builtin_amdgcn_readfirstlane(ldst);
  unsigned keep;
  if (SC1)
    asm volatile("s_mov_b32 %0, m0\n\ts_mov_b32 m0, %3\n\ts_nop 4\n\tglobal_load_lds_dwordx4 %1, %2 sc1\n\ts_mov_b32 m0, %0"
                 : "=&s"(keep) : "v"(voff), "s"(ub), "s"(ul) : "memory");
  else
    asm volatile("s_mov_b32 %0, m0\n\ts_mov_b32 m0, %3\n\ts_nop 4\n\tglobal_load_lds_dwordx4 %1, %2\n\ts_mov_b32 m0, %0"
                 : "=&s"(keep) : "v"(voff), "s"(ub), "s"(ul) : "memory");
}
__device__ __forceinline__ s16x8 tr_frag(const char* p0, const char* p1) {
  s16x4 a = __builtin_amdgcn_ds_read_tr16_b64_v4i16((lds_s16x4_ptr)p0);
  s16x4 b = __builtin_amdgcn_ds_read_tr16_b64_v4i16((lds_s16x4_ptr)p1);
  return __builtin_shufflevector(a, b, 0, 1, 2, 3, 4, 5, 6, 7);
}


#define RLX_AGENT __ATOMIC_RELAXED, __HIP_MEMORY_SCOPE_AGENT
typedef __attribute__((address_space(1))) unsigned gu32;
typedef __attribute__((address_space(1))) unsigned long long gu64;
__device__ __forceinline__ void st16_sc1(void* p, u32x4 v) { asm volatile("global_store_dwordx4 %0, %1, off sc1\n\ts_nop 1" ::"v"(p), "v"(v) : "memory"); }
__device__ __forceinline__ void st8_sc1(void* p, u32x2 v) { asm volatile("global_store_dwordx2 %0, %1, off sc1\n\ts_nop 1" ::"v"(p), "v"(v) : "memory"); }
__device__ __forceinline__ void wg_signal(unsigned* cnt) {
  asm volatile("s_waitcnt vmcnt(0) lgkmcnt(0)" ::: "memory");
  __builtin_amdgcn_s_barrier();
  if (threadIdx.x == 0) __hip_atomic_fetch_add((gu32*)cnt, 1u, RLX_AGENT);
}
__device__ __forceinline__ void wg_wait(unsigned* cnt, unsigned want) {
  if (threadIdx.x < 64) {
    unsigned spins = 0;
    while ((unsigned)__builtin_amdgcn_readfirstlane(__hip_atomic_load((gu32*)cnt, RLX_AGENT)) < want) {
      __builtin_amdgcn_s_sleep(1);
      if (++spins > (1u << 22)) break;
    }
  }
  asm volatile("" ::: "memory");
  __builtin_amdgcn_s_barrier();
  asm volatile("" ::: "memory");
}

#define SCHED() __builtin_amdgcn_sched_barrier(0)

__device__ __forceinline__ unsigned quad_get(unsigned v, int sel) {
  switch (sel) {
    case 1: return __builtin_amdgcn_update_dpp(0, (int)v, 0x55, 0xf, 0xf, true);
    case 2: return __builtin_amdgcn_update_dpp(0, (int)v, 0xAA, 0xf, 0xf, true);
    default: return __builtin_amdgcn_update_dpp(0, (int)v, 0xFF, 0xf, 0xf, true);
  }
}
template <int MODE>
__device__ __forceinline__ void row_phase(char* lds, int t0, int w, int lane, const u16* __restrict__ xin,
                                          const u16* __restrict__ YT, u16* __restrict__ Y,
                                          u16* __restrict__ xout, u16* __restrict__ xn) {
  u16 (*yt)[D + 8] = (u16 (*)[D + 8])lds;
  const int t = t0 + (w & 7);
  u32x4 xa = {0u, 0u, 0u, 0u}; unsigned xb = 0u;
  if (w < 8) {
    if (MODE == 0) {
      xa = *(const u32x4*)(xin + (size_t)t * D + 8 * lane);
      xb = *(const unsigned*)(xin + (size_t)t * D + 512 + 2 * lane);
    } else {
      gu64* xp = (gu64*)(xin + (size_t)t * D + 8 * lane);
      const unsigned long long v0 = __hip_atomic_load(xp, RLX_AGENT), v1 = __hip_atomic_load(xp + 1, RLX_AGENT);
      xa = (u32x4){(unsigned)v0, (unsigned)(v0 >> 32), (unsigned)v1, (unsigned)(v1 >> 32)};
      xb = __hip_atomic_load((gu32*)(xin + (size_t)t * D + 512 + 2 * lane), RLX_AGENT);
    }
  }
  u32x4 ya; unsigned yb;
  if (MODE == 0) {
    const int dd = w * 64 + lane;
    if (dd < D) {
      u32x4 q = *(const u32x4*)(YT + (size_t)dd * S + t0);
#pragma unroll
      for (int e = 0; e < 4; ++e) {
        yt[2 * e][dd] = (u16)(q[e] & 0xffffu);
        yt[2 * e + 1][dd] = (u16)(q[e] >> 16);
      }
    }
    __syncthreads();
  }
  if (w < 8) {
    if (MODE == 0) {
      ya = *(const u32x4*)&yt[w][8 * lane];
      yb = *(const unsigned*)&yt[w][512 + 2 * lane];
      *(u32x4*)(Y + (size_t)t * D + 8 * lane) = ya;
      *(unsigned*)(Y + (size_t)t * D + 512 + 2 * lane) = yb;
    } else {
      ya = *(const u32x4*)(Y + (size_t)t * D + 8 * lane);
      yb = *(const unsigned*)(Y + (size_t)t * D + 512 + 2 * lane);
    }
    f32x2 x[5] = {unpk(xa[0]), unpk(xa[1]), unpk(xa[2]), unpk(xa[3]), unpk(xb)};
    const unsigned yp[5] = {ya[0], ya[1], ya[2], ya[3], yb};
    unsigned n[5], xo[5];
    ln_row(x, n);
#pragma unroll
    for (int i = 0; i < 5; ++i) { xo[i] = cvtpk(x[i] + rbf2(unpk(n[i]) * unpk(yp[i]))); x[i] = unpk(xo[i]); }
    ln_row(x, n);
    const u32x4 xt = {xo[4], quad_get(xo[4], 1), quad_get(xo[4], 2), quad_get(xo[4], 3)};
    const u32x4 nt4 = {n[4], quad_get(n[4], 1), quad_get(n[4], 2), quad_get(n[4], 3)};
    const int sw = (t >> 1) & 7;
    u16* bp = xn + (size_t)(t >> 8) * (10 * 256 * 64) + (t & 255) * 64;
    st16_sc1(xout + (size_t)t * D + 8 * lane, (u32x4){xo[0], xo[1], xo[2], xo[3]});
    st16_sc1(bp + (lane >> 3) * (256 * 64) + (((lane & 7) ^ sw) * 8), (u32x4){n[0], n[1], n[2], n[3]});
    if ((lane & 3) == 0) {
      st16_sc1(xout + (size_t)t * D + 512 + 2 * lane, xt);
      st16_sc1(bp + (8 + (lane >> 5)) * (256 * 64) + ((((lane & 31) >> 2) ^ sw) * 8), nt4);
    }
  }
}

constexpr int G1_A = 256 * 64 * 2, G1_B = 64 * 128 * 2, G1_ST = G1_A + G1_B, G1_NS = 3;

__device__ __forceinline__ unsigned g1_boff(int nt, int lw, int i, int lane) {
  const int row = ((lw & 1) * 4 + i) * 8 + (lane >> 3);
  const int ch = (lane & 7) ^ (((row >> 1) & 1) << 2) ^ (((row >> 3) & 1) << 1);
  return (unsigned)(row * F + nt * 64 + ch * 8) * 2;
}
__device__ __forceinline__ void gemm1_pre(char* lds, int nt, int w, int lane, const u16* __restrict__ wg, const u16* __restrict__ wu) {
  if (w >= 8) {
    const int lw = w - 8;
    const unsigned lbase = lds_addr(lds);
    const char* bmat = (const char*)((lw & 2) ? wu : wg);
#pragma unroll
    for (int st = 0; st < 3; ++st)
#pragma unroll
      for (int i = 0; i < 4; ++i) glds16(bmat + (size_t)st * 64 * F * 2, g1_boff(nt, lw, i, lane), lbase + st * G1_ST + G1_A + (lw * 4 + i) * 1024);
  }
}
__device__ __forceinline__ void gemm1_phase(char* lds, int mt, int nt, int w, int lane, const u16* __restrict__ xn,
                                            const u16* __restrict__ wg, const u16* __restrict__ wu, u16* __restrict__ hout) {
  constexpr int NT = D / 64;
  if (w >= 8) {
    const int lw = w - 8;
    const unsigned lbase = lds_addr(lds);
    const unsigned voff = lane * 16;
    const char* abase = (const char*)xn + (size_t)mt * 10 * 32768 + lw * 8192;
    const char* bmat = (const char*)((lw & 2) ? wu : wg);
    unsigned boffs[4];
#pragma unroll
    for (int i = 0; i < 4; ++i) boffs[i] = g1_boff(nt, lw, i, lane);
    auto stageA = [&](int buf, int kt) {
#pragma unroll
      for (int i = 0; i < 8; ++i) glds16<true>(abase + kt * 32768 + i * 1024, voff, lbase + buf * G1_ST + (lw * 8 + i) * 1024);
    };
    auto stageB = [&](int buf, int kt) {
#pragma unroll
      for (int i = 0; i < 4; ++i) glds16(bmat + (size_t)kt * 64 * F * 2, boffs[i], lbase + buf * G1_ST + G1_A + (lw * 4 + i) * 1024);
    };
    stageA(0, 0);
    stageA(1, 1);
    WAIT_V(8);
    __builtin_amdgcn_s_barrier();
    stageA(2, 2);
    WAIT_V(8);
    __builtin_amdgcn_s_barrier();
    stageB(0, 3); stageA(0, 3);
    int buf = 1;
#pragma unroll 1
    for (int kt = 1; kt + 3 < NT; ++kt) {
      WAIT_V(12);
      __builtin_amdgcn_s_barrier();
      stageB(buf, kt + 3); stageA(buf, kt + 3);
      buf = (buf == 2) ? 0 : buf + 1;
    }
    WAIT_V(12);
    __builtin_amdgcn_s_barrier();
    WAIT_V(0);
    __builtin_amdgcn_s_barrier();
  } else {
    const int wm = w >> 1, wn = w & 1;
    f32x4 ag[4][2] = {}, au[4][2] = {};
    const int r = lane & 15, g4 = lane >> 4;
    const int arow = (wm * 64 + r) * 128;
    const int asw = (r >> 1) & 7;
    const int q = (lane & 15) >> 2, p = lane & 3;
    const int bsw = (((q >> 1) & 1) << 2) ^ ((g4 & 1) << 1);
    const int boff0 = G1_A + (8 * g4 + q) * 128 + (((4 * wn + (p >> 1)) ^ bsw) * 16) + (p & 1) * 8;
    const int boff1 = G1_A + (8 * g4 + q) * 128 + (((4 * wn + 2 + (p >> 1)) ^ bsw) * 16) + (p & 1) * 8;
    struct Frag { s16x8 a[4], bg[2], bu[2]; };
    auto ldfrag = [&](const char* base, int ks) -> Frag {
      Frag f;
      const char* ap = base + arow + (((4 * ks + g4) ^ asw) * 16);
      const char* b0 = base + boff0 + ks * 4096;
      const char* b1 = base + boff1 + ks * 4096;
      f.bg[0] = tr_frag(b0, b0 + 512);
      f.a[0] = *(const s16x8*)ap;
      f.bu[0] = tr_frag(b0 + 8192, b0 + 8192 + 512);
      f.a[1] = *(const s16x8*)(ap + 2048);
      f.bg[1] = tr_frag(b1, b1 + 512);
      f.a[2] = *(const s16x8*)(ap + 4096);
      f.bu[1] = tr_frag(b1 + 8192, b1 + 8192 + 512);
      f.a[3] = *(const s16x8*)(ap + 6144);
      return f;
    };
    auto mma = [&](const Frag& f) {
#pragma unroll
      for (int mi = 0; mi < 4; ++mi)
#pragma unroll
        for (int ci = 0; ci < 2; ++ci) {
          ag[mi][ci] = __builtin_amdgcn_mfma_f32_16x16x32_bf16(f.bg[ci], f.a[mi], ag[mi][ci], 0, 0, 0);
          au[mi][ci] = __builtin_amdgcn_mfma_f32_16x16x32_bf16(f.bu[ci], f.a[mi], au[mi][ci], 0, 0, 0);
        }
    };
#define ILV16()                                                                                       \
  do {                                                                                                \
    _Pragma("unroll") for (int i_ = 0; i_ < 12; ++i_) {                                               \
      __builtin_amdgcn_sched_group_barrier(0x008, 1, 0);                                              \
      __builtin_amdgcn_sched_group_barrier(0x100, 1, 0);                                              \
    }                                                                                                 \
    __builtin_amdgcn_sched_group_barrier(0x008, 4, 0);                                                \
  } while (0)
    asm volatile("" ::: "memory");
    __builtin_amdgcn_s_barrier();
    asm volatile("" ::: "memory");
    Frag f0_ = ldfrag(lds, 0);
    int buf = 0;
#pragma unroll 1
    for (int kt = 0; kt < NT - 1; ++kt) {
      const char* base = lds + buf * G1_ST;
      SCHED();
      Frag f1_ = ldfrag(base, 1);
      mma(f0_);
      ILV16();
      SCHED();
      buf = (buf == 2) ? 0 : buf + 1;
      WAIT_L(0);
      __builtin_amdgcn_s_barrier();
      asm volatile("" ::: "memory");
      SCHED();
      f0_ = ldfrag(lds + buf * G1_ST, 0);
      mma(f1_);
      ILV16();
      SCHED();
    }
    {
      const char* base = lds + buf * G1_ST;
      Frag f1_ = ldfrag(base, 1);
      mma(f0_);
      ILV16();
      SCHED();
      mma(f1_);
    }
    constexpr int HROW = 80;
    char* slab = lds + G1_ST + w * (64 * HROW);
    u16* hbase = hout + ((size_t)(mt * 4 + wm) * 27 + nt) * 4096;
#pragma unroll
    for (int mh = 0; mh < 2; ++mh) {
#pragma unroll
      for (int m2 = 0; m2 < 2; ++m2) {
        const int mi = mh * 2 + m2;
#pragma unroll
        for (int ci = 0; ci < 2; ++ci) {
          u32x2 o;
#pragma unroll
          for (int e = 0; e < 2; ++e) {
            const f32x2 gv = rbf2((f32x2){ag[mi][ci][2 * e], ag[mi][ci][2 * e + 1]}) * -1.44269504f;
            const f32x2 uv = rbf2((f32x2){au[mi][ci][2 * e], au[mi][ci][2 * e + 1]});
            const f32x2 den = (f32x2){__builtin_amdgcn_exp2f(gv[0]), __builtin_amdgcn_exp2f(gv[1])} + 1.0f;
            const f32x2 sg = rbf2((f32x2){__builtin_amdgcn_rcpf(den[0]), __builtin_amdgcn_rcpf(den[1])});
            o[e] = cvtpk(sg * uv);
          }
          *(u32x2*)(slab + (mi * 16 + r) * HROW + ci * 32 + g4 * 8) = o;
        }
      }
      asm volatile("s_waitcnt lgkmcnt(0)" ::: "memory");
#pragma unroll
      for (int i = 0; i < 2; ++i) {
        const int row = mh * 32 + i * 16 + (lane >> 2), c = lane & 3;
        u32x4 o = *(const u32x4*)(slab + row * HROW + c * 16);
        st16_sc1(hbase + row * 64 + (((wn * 4 + c) ^ ((row >> 1) & 7)) * 8), o);
      }
    }
  }
}

constexpr int G2_A = 64 * 64 * 2, G2_B = 64 * 80 * 2, G2_ST = G2_A + G2_B, G2_NS = 6, G2_LAG = 1;

__device__ __forceinline__ void gemm2_pre(char* lds, int nt, int w, int lane, const u16* __restrict__ wpk) {
  if (w >= 4) {
    const int lw = w - 4;
    const unsigned lbase = lds_addr(lds);
    const char* bbase = (const char*)wpk + (size_t)nt * 27 * 10240 + lw * 1024;
#pragma unroll
    for (int st = 0; st < G2_NS; ++st) {
      glds16(bbase + st * 10240, lane * 16, lbase + st * G2_ST + G2_A + lw * 1024);
      if (lw < 2) glds16(bbase + st * 10240 + 8192, lane * 16, lbase + st * G2_ST + G2_A + (lw + 8) * 1024);
    }
  }
}
template <bool HANDOFF>
__device__ __forceinline__ void gemm2_phase(char* lds, int mt, int nt, int w, int lane, const u16* __restrict__ hin,
                                            const u16* __restrict__ wpk, const u16* __restrict__ xres,
                                            u16* __restrict__ out) {
  constexpr int NT = F / 64;
  const int m0 = mt * 64, n0 = nt * 80;
  if (w >= 4) {
    const int lw = w - 4;
    const unsigned lbase = lds_addr(lds);
    const unsigned voff = lane * 16;
    const char* abase = (const char*)hin + (size_t)mt * 27 * 8192 + lw * 1024;
    const char* bbase = (const char*)wpk + (size_t)nt * 27 * 10240 + lw * 1024;
    auto stageA = [&](int buf, int kt) { glds16<true>(abase + kt * 8192, voff, lbase + buf * G2_ST + lw * 1024); };
    auto stage = [&](int buf, int kt) {
      const unsigned a = lbase + buf * G2_ST;
      glds16<true>(abase + kt * 8192, voff, a + lw * 1024);
      glds16(bbase + kt * 10240, voff, a + G2_A + lw * 1024);
      if (lw < 2) glds16(bbase + kt * 10240 + 8192, voff, a + G2_A + (lw + 8) * 1024);
    };
    stageA(0, 0);
    stageA(1, 1);
    WAIT_V(1);
    __builtin_amdgcn_s_barrier();
#pragma unroll
    for (int i = 2; i < G2_NS; ++i) stageA(i, i);
    auto loader_loop = [&](auto per_tile_c) {
      constexpr int PT = decltype(per_tile_c)::value;
      WAIT_V(4); __builtin_amdgcn_s_barrier();
      WAIT_V(3); __builtin_amdgcn_s_barrier(); stage(0, 6);
      WAIT_V(2 + PT); __builtin_amdgcn_s_barrier(); stage(1, 7);
      WAIT_V(1 + 2 * PT); __builtin_amdgcn_s_barrier(); stage(2, 8);
      int buf = 3;
#pragma unroll 1
      for (int kt = 4; kt - 1 + G2_NS < NT; ++kt) {
        WAIT_V(PT * 3);
        __builtin_amdgcn_s_barrier();
        stage(buf, kt - 1 + G2_NS);
        buf = (buf == G2_NS - 1) ? 0 : buf + 1;
      }
#pragma unroll
      for (int i = G2_NS - 2; i >= 1; --i) { WAIT_V(PT * (i - 1)); __builtin_amdgcn_s_barrier(); }
    };
    if (lw < 2) loader_loop(std::integral_constant<int, 3>{}); else loader_loop(std::integral_constant<int, 2>{});
    __builtin_amdgcn_s_barrier();
    __builtin_amdgcn_s_barrier();
  } else {
    const int wm = w & 1, kh = w >> 1;
    f32x4 acc[2][5] = {};
    const int r = lane & 15, g4 = lane >> 4;
    const int aoff0 = (wm * 32 + r) * 128 + (((4 * kh + g4) ^ ((r >> 1) & 7)) * 16);
    const int q = (lane & 15) >> 2, p = lane & 3;
    const int boff = G2_A + (32 * kh + 8 * g4 + q) * 160 + p * 8;
    struct Frag { s16x8 a[2], b[5]; };
    auto ldfrag = [&](const char* base) -> Frag {
      Frag f;
      f.a[0] = *(const s16x8*)(base + aoff0);
      f.a[1] = *(const s16x8*)(base + aoff0 + 2048);
#pragma unroll
      for (int c = 0; c < 5; ++c) f.b[c] = tr_frag(base + boff + c * 32, base + boff + c * 32 + 640);
      return f;
    };
    auto mma = [&](const Frag& f) {
#pragma unroll
      for (int c = 0; c < 5; ++c) {
        acc[0][c] = __builtin_amdgcn_mfma_f32_16x16x32_bf16(f.b[c], f.a[0], acc[0][c], 0, 0, 0);
        acc[1][c] = __builtin_amdgcn_mfma_f32_16x16x32_bf16(f.b[c], f.a[1], acc[1][c], 0, 0, 0);
      }
    };
    u32x2 xr[2][5];
    if (kh == 0) {
#pragma unroll
      for (int mi = 0; mi < 2; ++mi)
#pragma unroll
        for (int c = 0; c < 5; ++c) {
          const unsigned long long v = __hip_atomic_load((gu64*)(xres + (size_t)(m0 + wm * 32 + mi * 16 + r) * D + n0 + c * 16 + 4 * g4), RLX_AGENT);
          xr[mi][c] = (u32x2){(unsigned)v, (unsigned)(v >> 32)};
        }
    }
    asm volatile("" ::: "memory");
    __builtin_amdgcn_s_barrier();
    asm volatile("" ::: "memory");
    Frag fa = ldfrag(lds);
    int buf = 0;
#pragma unroll 1
    for (int kt = 0; kt < NT - 1; kt += 2) {
      buf = (buf == G2_NS - 1) ? 0 : buf + 1;
      __builtin_amdgcn_s_barrier();
      asm volatile("" ::: "memory");
      SCHED();
      Frag fb = ldfrag(lds + buf * G2_ST);
      mma(fa);
#pragma unroll
      for (int i = 0; i < 6; ++i) { __builtin_amdgcn_sched_group_barrier(0x008, 1, 0); __builtin_amdgcn_sched_group_barrier(0x100, 2, 0); }
      __builtin_amdgcn_sched_group_barrier(0x008, 4, 0);
      SCHED();
      buf = (buf == G2_NS - 1) ? 0 : buf + 1;
      __builtin_amdgcn_s_barrier();
      asm volatile("" ::: "memory");
      SCHED();
      fa = ldfrag(lds + buf * G2_ST);
      mma(fb);
#pragma unroll
      for (int i = 0; i < 6; ++i) { __builtin_amdgcn_sched_group_barrier(0x008, 1, 0); __builtin_amdgcn_sched_group_barrier(0x100, 2, 0); }
      __builtin_amdgcn_sched_group_barrier(0x008, 4, 0);
      SCHED();
    }
    mma(fa);
    WAIT_L(0);
    __builtin_amdgcn_s_barrier();
    asm volatile("" ::: "memory");
    constexpr int PROW = 336;
    if (kh == 1) {
#pragma unroll
      for (int mi = 0; mi < 2; ++mi)
#pragma unroll
        for (int c = 0; c < 5; ++c)
          *(f32x4*)(lds + (wm * 32 + mi * 16 + r) * PROW + (c * 16 + 4 * g4) * 4) = acc[mi][c];
    }
    WAIT_L(0);
    __builtin_amdgcn_s_barrier();
    asm volatile("" ::: "memory");
    if (kh == 0) {
#pragma unroll
      for (int mi = 0; mi < 2; ++mi) {
        const int row = m0 + wm * 32 + mi * 16 + r;
#pragma unroll
        for (int c = 0; c < 5; ++c) {
          f32x4 o = *(const f32x4*)(lds + (wm * 32 + mi * 16 + r) * PROW + (c * 16 + 4 * g4) * 4);
          const size_t idx = (size_t)row * D + n0 + c * 16 + 4 * g4;
          const u32x2 xv = xr[mi][c];
          u32x2 ov = {cvtpk(unpk(xv[0]) + rbf2((f32x2){acc[mi][c][0] + o[0], acc[mi][c][1] + o[1]})),
                      cvtpk(unpk(xv[1]) + rbf2((f32x2){acc[mi][c][2] + o[2], acc[mi][c][3] + o[3]}))};
          if (HANDOFF) st8_sc1(out + idx, ov); else *(u32x2*)(out + idx) = ov;
        }
      }
    }
  }
}

constexpr int CNT_STRIDE = 64;
constexpr int CNT_A = 0, CNT_B = 8, CNT_C = 16, CNT_D = 48, CNT_E = 56, CNT_N = 64;

struct MainArgs {
  const u16* q; const u16* YT; u16* Y; u16* x1; u16* x2; u16* x3; u16* xn1; u16* xn2; u16* h1; u16* h2;
  const u16* wg; const u16* wu; const u16* wpk2; u16* out; unsigned* cnt;
};

__global__ __launch_bounds__(768) void k_main(MainArgs a) {
  __shared__ __attribute__((aligned(1024))) char lds[G1_NS * G1_ST];
  const int id = blockIdx.x;
  const int g = id & 7, j = id >> 3;
  const int tid = threadIdx.x, lane = tid & 63;
  const int w = __builtin_amdgcn_readfirstlane(tid >> 6);
  const int p64 = g * 4 + (j & 3), n2 = j >> 2;
  const int t0 = p64 * 64 + n2 * 8;
  const int pm = p64 >> 2;
  const int mt1 = (g & 1) * 4 + (j & 3), nj = j >> 2, nt1 = (g >> 1) * 7 + nj;
  const bool has1 = (nj < 7) && (nt1 < 27);
  unsigned* cnt = a.cnt;

  row_phase<0>(lds, t0, w, lane, a.q, a.YT, a.Y, a.x1, a.xn1);
  wg_signal(cnt + (CNT_A + pm) * CNT_STRIDE);
  if (has1) {
    gemm1_pre(lds, nt1, w, lane, a.wg, a.wu);
    wg_wait(cnt + (CNT_A + mt1) * CNT_STRIDE, 32);
    gemm1_phase(lds, mt1, nt1, w, lane, a.xn1, a.wg, a.wu, a.h1);
    wg_signal(cnt + (CNT_B + mt1) * CNT_STRIDE);
  }
  gemm2_pre(lds, n2, w, lane, a.wpk2);
  wg_wait(cnt + (CNT_B + pm) * CNT_STRIDE, 27);
  gemm2_phase<true>(lds, p64, n2, w, lane, a.h1, a.wpk2, a.x1, a.x2);
  wg_signal(cnt + (CNT_C + p64) * CNT_STRIDE);
  wg_wait(cnt + (CNT_C + p64) * CNT_STRIDE, 8);
  row_phase<1>(lds, t0, w, lane, a.x2, a.YT, a.Y, a.x3, a.xn2);
  wg_signal(cnt + (CNT_D + pm) * CNT_STRIDE);
  if (has1) {
    gemm1_pre(lds, nt1, w, lane, a.wg, a.wu);
    wg_wait(cnt + (CNT_D + mt1) * CNT_STRIDE, 32);
    gemm1_phase(lds, mt1, nt1, w, lane, a.xn2, a.wg, a.wu, a.h2);
    wg_signal(cnt + (CNT_E + mt1) * CNT_STRIDE);
  }
  gemm2_pre(lds, n2, w, lane, a.wpk2);
  wg_wait(cnt + (CNT_E + pm) * CNT_STRIDE, 27);
  gemm2_phase<false>(lds, p64, n2, w, lane, a.h2, a.wpk2, a.x3, a.out);
}

extern "C" void kernel_launch(void* const* d_in, const int* in_sizes, int n_in, void* d_out, int out_size,
                              void* d_ws, size_t ws_size, hipStream_t stream) {
  const u16* q = (const u16*)d_in[0];
  const u16* k = (const u16*)d_in[1];
  const u16* v = (const u16*)d_in[2];
  const u16* shift_k = (const u16*)d_in[3];
  const u16* ssm_k = (const u16*)d_in[4];
  const u16* ssm_D = (const u16*)d_in[5];
  const u16* wg = (const u16*)d_in[7];
  const u16* wu = (const u16*)d_in[8];
  const u16* wd = (const u16*)d_in[9];
  char* ws = (char*)d_ws;
  constexpr size_t SZ = (size_t)S * D * 2;
  u16* kvT = (u16*)(ws + 0 * SZ);
  u16* YT = (u16*)(ws + 1 * SZ);
  u16* wpk2 = (u16*)(ws + 12 * SZ);
  MainArgs a;
  a.q = q; a.YT = YT; a.Y = (u16*)(ws + 2 * SZ);
  a.x1 = (u16*)(ws + 3 * SZ); a.xn1 = (u16*)(ws + 4 * SZ); a.x2 = (u16*)(ws + 5 * SZ); a.x3 = (u16*)(ws + 6 * SZ);
  a.h1 = (u16*)(ws + 7 * SZ); a.xn2 = (u16*)(ws + 13 * SZ); a.h2 = (u16*)(ws + 14 * SZ);
  a.wg = wg; a.wu = wu; a.wpk2 = wpk2; a.out = (u16*)d_out; a.cnt = (unsigned*)(ws + 17 * SZ);

  k_prep<<<320 + PK_WGS, 256, 0, stream>>>(k, v, shift_k, kvT, wd, wpk2, a.cnt);
  k_conv<<<D, 256, 0, stream>>>(kvT, ssm_k, ssm_D, YT);
  k_main<<<256, 768, 0, stream>>>(a);
}
```
